```python
import torch

def _softmax(x, axis=-1):
    return torch.softmax(x, dim=axis)
def _gelu(x):
    return torch.nn.functional.gelu(x, approximate="tanh")
def _silu(x):
    return torch.nn.functional.silu(x)
def _sigmoid(x):
    return torch.sigmoid(x)
def _layernorm(x, axis=-1, eps=1e-5):
    m = x.mean(dim=axis, keepdim=True)
    v = x.var(dim=axis, keepdim=True, unbiased=False)
    return (x - m) / torch.sqrt(v + eps)
def _scatter_add(tgt, idx, src, axis=0):
    return tgt.clone().index_add_(axis, idx, src)
def _segment_sum(src, seg, n):
    out = torch.zeros((n,) + src.shape[1:], dtype=src.dtype)
    return out.index_add_(0, seg, src)
def _pad(x, pw):
    flat = tuple(v for pair in reversed(pw) for v in pair)
    return torch.nn.functional.pad(x, flat)

kernel_name = 'sequential_preLN__gqa_block_cached__geglu_ffn__decode'

def setup_inputs():

    _w = (lambda a: (lambda t: t.to(torch.bfloat16) if t.is_floating_point() else t)(torch.as_tensor(a)))
    def _A_setup():
        B, Sq, D = 8, 1, 576
        Hq, Hkv, Sk, Dh = 32, 16, 4096, 128
        assert Hq % Hkv == 0, (Hq, Hkv)
        return {
            "x": _w((lambda *s: torch.randn(*s, dtype=torch.bfloat16) * 0.05)(B, Sq, D)),
            "k_cache": _w((lambda *s: torch.randn(*s, dtype=torch.bfloat16) * 3.2)(B, Hkv, Sk, Dh)),
            "v_cache": _w((lambda *s: torch.randn(*s, dtype=torch.bfloat16) * 0.4)(B, Hkv, Sk, Dh)),
            "wq": _w((lambda *s: torch.randn(*s, dtype=torch.bfloat16) * 3.2)(D, Hq * Dh) * (D ** -0.5)),
            "wo": _w((lambda *s: torch.randn(*s, dtype=torch.bfloat16) * 0.4)(Hq * Dh, D) * ((Hq * Dh) ** -0.5)),
        }
    _A_in = _A_setup()

    def _B_setup():
        B, S, D, F = 8, 1, 576, 4608
        return {
            "x": _w((lambda *s: torch.randn(*s, dtype=torch.bfloat16) * 0.05)(B, S, D)),
            "w_gate": _w((lambda *s: torch.randn(*s, dtype=torch.bfloat16) * 0.05)(D, F)),
            "w_up": _w((lambda *s: torch.randn(*s, dtype=torch.bfloat16) * 0.05)(D, F)),
            "w_down": _w((lambda *s: torch.randn(*s, dtype=torch.bfloat16) * 0.05)(F, D)),
        }
    _B_in = _B_setup()

    return {
        "A_x": _A_in["x"],
        "A_k_cache": _A_in["k_cache"],
        "A_v_cache": _A_in["v_cache"],
        "A_wq": _A_in["wq"],
        "A_wo": _A_in["wo"],
        "B_x": _B_in["x"],
        "B_w_gate": _B_in["w_gate"],
        "B_w_up": _B_in["w_up"],
        "B_w_down": _B_in["w_down"],
    }

def reference(A_x, A_k_cache, A_v_cache, A_wq, A_wo, B_x, B_w_gate, B_w_up, B_w_down):
    def _A_ref(x, k_cache, v_cache, wq, wo):
        B, Sq, D = x.shape
        Hq, Dh = 32, 128
        Hkv = k_cache.shape[1]
        rep = Hq // Hkv
        q = (lambda x, *d: x.permute(*d))((x @ wq).reshape(B, Sq, Hq, Dh), 0, 2, 1, 3)
        k = (lambda x, n, axis: torch.repeat_interleave(x, n, dim=axis))(k_cache, rep, axis=1)
        v = (lambda x, n, axis: torch.repeat_interleave(x, n, dim=axis))(v_cache, rep, axis=1)
        scores = torch.einsum("bhqd,bhkd->bhqk", q, k) * (Dh ** -0.5)
        p = _softmax(scores, axis=-1)
        o = torch.einsum("bhqk,bhkd->bhqd", p, v)
        return (lambda x, *d: x.permute(*d))(o, 0, 2, 1, 3).reshape(B, Sq, Hq * Dh) @ wo

    def _B_ref(x, w_gate, w_up, w_down):
        return (_gelu(x @ w_gate) * (x @ w_up)) @ w_down

    x = A_x
    h = x + _A_ref(_layernorm(x, axis=-1), A_k_cache, A_v_cache, A_wq, A_wo)
    h2 = h + _B_ref(_layernorm(h, axis=-1), B_w_gate, B_w_up, B_w_down)
    return h2
```

```cpp
#include <hip/hip_runtime.h>
#include <hip/hip_bf16.h>
#include <cstdint>
#define EMU 0
typedef unsigned short u16;
constexpr int B = 8, D = 576, HQ = 32, HKV = 16, SK = 4096, DH = 128, F = 4608;
__device__ __forceinline__ float bf2f(u16 v) { return __uint_as_float((unsigned)v << 16); }
__device__ __forceinline__ u16 f2bf(float f) { __hip_bfloat16 b = __float2bfloat16(f); return *reinterpret_cast<u16*>(&b); }
__device__ __forceinline__ float rb(float f) { return EMU ? bf2f(f2bf(f)) : f; }

__device__ void ln_row(const float* in, float* out, float* red) {
  int t = threadIdx.x;
  float s = 0.f; for (int i = t; i < D; i += 256) s += in[i];
  red[t] = s; __syncthreads();
  for (int o = 128; o > 0; o >>= 1) { if (t < o) red[t] += red[t + o]; __syncthreads(); }
  float mean = red[0] / D; __syncthreads();
  float v = 0.f; for (int i = t; i < D; i += 256) { float d = in[i] - mean; v += d * d; }
  red[t] = v; __syncthreads();
  for (int o = 128; o > 0; o >>= 1) { if (t < o) red[t] += red[t + o]; __syncthreads(); }
  float var = red[0] / D; __syncthreads();
  float m = rb(mean), vv = rb(var);
  float den = rb(sqrtf(rb(vv + 1e-5f)));
  for (int i = t; i < D; i += 256) out[i] = rb(rb(in[i] - m) / den);
  __syncthreads();
}

__global__ void __launch_bounds__(256) k_q(const u16* x, const u16* wq, float* q) {
  __shared__ float xin[D], xn[D], red[256];
  int b = blockIdx.y;
  for (int i = threadIdx.x; i < D; i += 256) xin[i] = bf2f(x[b * D + i]);
  __syncthreads();
  ln_row(xin, xn, red);
  int col = blockIdx.x * 256 + threadIdx.x;
  float acc = 0.f;
  for (int k = 0; k < D; ++k) acc += xn[k] * bf2f(wq[(size_t)k * (HQ * DH) + col]);
  q[b * HQ * DH + col] = rb(acc);
}

__global__ void __launch_bounds__(256) k_attn(const u16* kc, const u16* vc, const float* q, float* o) {
  __shared__ float sc[SK], qs[DH], red[256], oacc[2][DH];
  int h = blockIdx.x, b = blockIdx.y, kvh = h / 2, t = threadIdx.x;
  if (t < DH) qs[t] = q[b * HQ * DH + h * DH + t];
  __syncthreads();
  const u16* K = kc + (size_t)(b * HKV + kvh) * SK * DH;
  const u16* V = vc + (size_t)(b * HKV + kvh) * SK * DH;
  float mx = -INFINITY;
  for (int k = t; k < SK; k += 256) {
    float acc = 0.f;
    const uint4* kr = reinterpret_cast<const uint4*>(K + (size_t)k * DH);
    for (int c = 0; c < 16; ++c) { uint4 w = kr[c]; unsigned ww[4] = {w.x, w.y, w.z, w.w};
      for (int j = 0; j < 4; ++j) { acc += qs[c * 8 + 2 * j] * __uint_as_float(ww[j] << 16); acc += qs[c * 8 + 2 * j + 1] * __uint_as_float(ww[j] & 0xffff0000u); } }
    float s = rb(rb(acc) * 0.08838834764831845f);
    sc[k] = s; mx = fmaxf(mx, s);
  }
  red[t] = mx; __syncthreads();
  for (int o2 = 128; o2 > 0; o2 >>= 1) { if (t < o2) red[t] = fmaxf(red[t], red[t + o2]); __syncthreads(); }
  mx = red[0]; __syncthreads();
  float sum = 0.f;
  for (int k = t; k < SK; k += 256) { float e = __expf(sc[k] - mx); sc[k] = e; sum += e; }
  red[t] = sum; __syncthreads();
  for (int o2 = 128; o2 > 0; o2 >>= 1) { if (t < o2) red[t] += red[t + o2]; __syncthreads(); }
  sum = red[0]; __syncthreads();
  int d = t & 127, half = t >> 7;
  float acc = 0.f;
  for (int k = half; k < SK; k += 2) acc += sc[k] * bf2f(V[(size_t)k * DH + d]);
  oacc[half][d] = acc; __syncthreads();
  if (t < DH) o[b * HQ * DH + h * DH + t] = rb((oacc[0][t] + oacc[1][t]) / sum);
}

__global__ void __launch_bounds__(256) k_wo(const u16* x, const u16* wo, const float* o, float* hbuf) {
  __shared__ float os[HQ * DH], red[256];
  int b = blockIdx.y, t = threadIdx.x;
  for (int i = t; i < HQ * DH; i += 256) os[i] = o[b * HQ * DH + i];
  __syncthreads();
  int col = blockIdx.x * 64 + (t & 63), kg = t >> 6;
  float acc = 0.f;
  for (int k = kg; k < HQ * DH; k += 4) acc += os[k] * bf2f(wo[(size_t)k * D + col]);
  red[t] = acc; __syncthreads();
  if (t < 64) { float a = red[t] + red[t + 64] + red[t + 128] + red[t + 192];
    hbuf[b * D + col] = rb(bf2f(x[b * D + col]) + rb(a)); }
}

__global__ void __launch_bounds__(256) k_gu(const float* hbuf, const u16* wg, const u16* wu, float* act) {
  __shared__ float xin[D], xn[D], red[256];
  int b = blockIdx.y;
  for (int i = threadIdx.x; i < D; i += 256) xin[i] = hbuf[b * D + i];
  __syncthreads();
  ln_row(xin, xn, red);
  int col = blockIdx.x * 256 + threadIdx.x;
  float g = 0.f, u = 0.f;
  for (int k = 0; k < D; ++k) { g += xn[k] * bf2f(wg[(size_t)k * F + col]); u += xn[k] * bf2f(wu[(size_t)k * F + col]); }
  g = rb(g); u = rb(u);
  float inner = 0.7978845608028654f * (g + 0.044715f * g * g * g);
  float ge = rb(0.5f * g * (1.f + tanhf(inner)));
  act[b * F + col] = rb(ge * u);
}

__global__ void __launch_bounds__(256) k_down(const float* hbuf, const u16* wd, const float* act, u16* out) {
  __shared__ float as[F], red[256];
  int b = blockIdx.y, t = threadIdx.x;
  for (int i = t; i < F; i += 256) as[i] = act[b * F + i];
  __syncthreads();
  int col = blockIdx.x * 64 + (t & 63), kg = t >> 6;
  float acc = 0.f;
  for (int k = kg; k < F; k += 4) acc += as[k] * bf2f(wd[(size_t)k * D + col]);
  red[t] = acc; __syncthreads();
  if (t < 64) { float a = red[t] + red[t + 64] + red[t + 128] + red[t + 192];
    out[b * D + col] = f2bf(hbuf[b * D + col] + rb(a)); }
}

extern "C" void kernel_launch(void* const* d_in, const int* in_sizes, int n_in, void* d_out, int out_size, void* d_ws, size_t ws_size, hipStream_t stream) {
  const u16* x = (const u16*)d_in[0]; const u16* kc = (const u16*)d_in[1]; const u16* vc = (const u16*)d_in[2];
  const u16* wq = (const u16*)d_in[3]; const u16* wo = (const u16*)d_in[4];
  const u16* wg = (const u16*)d_in[6]; const u16* wu = (const u16*)d_in[7]; const u16* wd = (const u16*)d_in[8];
  float* ws = (float*)d_ws;
  float* q = ws; float* o = q + B * HQ * DH; float* hbuf = o + B * HQ * DH; float* act = hbuf + B * D;
  k_q<<<dim3(16, 8), 256, 0, stream>>>(x, wq, q);
  k_attn<<<dim3(32, 8), 256, 0, stream>>>(kc, vc, q, o);
  k_wo<<<dim3(9, 8), 256, 0, stream>>>(x, wo, o, hbuf);
  k_gu<<<dim3(18, 8), 256, 0, stream>>>(hbuf, wg, wu, act);
  k_down<<<dim3(9, 8), 256, 0, stream>>>(hbuf, wd, act, (u16*)d_out);
}
```

```cpp
#include <hip/hip_runtime.h>
#include <hip/hip_bf16.h>

typedef unsigned short u16;
typedef unsigned long long u64;
typedef unsigned v4u __attribute__((ext_vector_type(4)));
typedef unsigned v2u __attribute__((ext_vector_type(2)));
typedef float f32x4 __attribute__((ext_vector_type(4)));
typedef short bf16x8 __attribute__((ext_vector_type(8)));
typedef short s16x4 __attribute__((ext_vector_type(4)));
typedef __bf16 bf16x2_t __attribute__((ext_vector_type(2)));
typedef float f32x2_t __attribute__((ext_vector_type(2)));
#define LAS __attribute__((address_space(3)))
#define GAS __attribute__((address_space(1)))
typedef __amdgpu_buffer_rsrc_t rsrc_t;

#define STAMPS 0
#define DELTA_F 4
#define DELTA_O 4
#define N_EARLY 144
#define DOWN_EARLY 0

constexpr int WS_EPOCH = 0;
constexpr int WS_DONE = 4096;
constexpr int WS_LSE = 8192;
constexpr int WS_H = 16384;
constexpr int WS_QG = 131072;
constexpr int WS_O = WS_QG + 8 * 4 * 4096 * 4;
constexpr int WS_P = WS_O + 32 * 16 * 512;
constexpr int WS_END = WS_P + 144 * 72 * 128;
constexpr int WS_DBG = 4 << 20;
constexpr int L_GATE = 0, L_UP = 73728, L_WO = 0, L_RED1 = 131072, L_WQ = 129024;
constexpr int L_XN = 147456, XN_STR = 1168, HN_STR = 1216;
constexpr int L_REC = L_XN, REC_STR = 1536;
constexpr int L_ACT = L_XN + 8 * HN_STR, ACT_STR = 144;
constexpr int L_RED = L_ACT + 8 * ACT_STR;
constexpr int L_Q = L_RED + 1152;
constexpr int L_FLG = L_Q + 512;
constexpr int L_END = L_FLG + 256;
static_assert(L_END <= 163840, "LDS");
constexpr int L_SCR = L_END;
constexpr int L_TOTAL = L_SCR + 2048 + 256;
static_assert(L_TOTAL <= 163840, "LDS");
constexpr int F_QREADY = 0, F_QDONE = 1, F_CHAIN = 2, F_TICK = 3, F_XN = 5, F_FULL = 6, F_TAG = 8, F_S0 = 10, F_FIN = 12, F_END = 14;
#define CODE_PF 3
#define TLB_PF 1
#define EXP 0
#define MAPV 0
#define PACE 0
#define TAU 56
#define T_TARGET 3900
#define LEAD_F 0
#define LEAD_O 0
constexpr u64 TAG_STEP = 0x9E3779ull << 40 | 0x16Bull << 30 | 0x2D5ull << 20 | 0x0F3ull << 10 | 0x1B5ull;
#define TAG_FIELDS(t) v4u{(unsigned)(t) & 0x3ffu, ((unsigned)(t) >> 10) & 0x3ffu, ((unsigned)(t) >> 20) & 0x3ffu, (unsigned)((t) >> 30) & 0x3ffu}
#define WQ_AHEAD 12
#define ROT_ON 1
#define WINDOW 9
#define WIN() asm volatile("s_waitcnt vmcnt(" STR(WINDOW) ")" ::: "memory")
#define PPOLL_AUX AUX_SC1
#define QPOLL_AUX AUX_SC1
#define QPOLL_DELAY 50
#define MAXL 8
#define DMA_INFL 12
#define STR2(x) #x
#define STR(x) STR2(x)

constexpr int AUX_NT = 2, AUX_SC1 = 16, AUX_SC1NT = 18;
constexpr unsigned OOB = 0x7FFFFFF0u;
constexpr int SPIN_CAP = 1 << 17, LDS_SPIN_CAP = 1 << 22;
constexpr float LOG2E = 1.4426950408889634f;

#define SB() __builtin_amdgcn_sched_barrier(0)
#define CBAR() asm volatile("" ::: "memory")
#define WG_BARRIER() do { asm volatile("s_waitcnt lgkmcnt(0)\n\ts_barrier" ::: "memory"); } while (0)

__device__ __forceinline__ f32x4 mfma32(v4u a, v4u b, f32x4 c) {
  return __builtin_amdgcn_mfma_f32_16x16x32_bf16(__builtin_bit_cast(bf16x8, a), __builtin_bit_cast(bf16x8, b), c, 0, 0, 0);
}
__device__ __forceinline__ f32x4 mfma16(v2u a, v2u b, f32x4 c) {
  return __builtin_amdgcn_mfma_f32_16x16x16bf16_1k(__builtin_bit_cast(s16x4, a), __builtin_bit_cast(s16x4, b), c, 0, 0, 0);
}
__device__ __forceinline__ unsigned cvtpk(float lo, float hi) {
  f32x2_t v = {lo, hi};
  return __builtin_bit_cast(unsigned, __builtin_convertvector(v, bf16x2_t));
}
__device__ __forceinline__ float rbf(float x) { return __uint_as_float(cvtpk(x, x) & 0xffff0000u); }
__device__ __forceinline__ float bflo(unsigned x) { return __uint_as_float(x << 16); }
__device__ __forceinline__ float bfhi(unsigned x) { return __uint_as_float(x & 0xffff0000u); }
template <int CTRL> __device__ __forceinline__ float dppf(float x) {
  return __builtin_bit_cast(float, __builtin_amdgcn_mov_dpp(__builtin_bit_cast(int, x), CTRL, 0xf, 0xf, true));
}
template <int CTRL> __device__ __forceinline__ unsigned dppu(unsigned x) { return (unsigned)__builtin_amdgcn_mov_dpp((int)x, CTRL, 0xf, 0xf, true); }
constexpr int QP_XOR1 = 0xB1, QP_XOR2 = 0x4E, ROR4 = 0x124, ROR8 = 0x128;
constexpr int QP_0011 = 0x50, QP_2233 = 0xFA, QP_0000 = 0x00, QP_1111 = 0x55, QP_ID = 0xE4;
__device__ __forceinline__ float xrow_max(float x) {
  auto s = __builtin_amdgcn_permlane16_swap(__float_as_uint(x), __float_as_uint(x), false, false);
  x = fmaxf(__uint_as_float(s[0]), __uint_as_float(s[1]));
  auto t = __builtin_amdgcn_permlane32_swap(__float_as_uint(x), __float_as_uint(x), false, false);
  return fmaxf(__uint_as_float(t[0]), __uint_as_float(t[1]));
}
__device__ __forceinline__ float xrow_sum(float x) {
  auto s = __builtin_amdgcn_permlane16_swap(__float_as_uint(x), __float_as_uint(x), false, false);
  x = __uint_as_float(s[0]) + __uint_as_float(s[1]);
  auto t = __builtin_amdgcn_permlane32_swap(__float_as_uint(x), __float_as_uint(x), false, false);
  return __uint_as_float(t[0]) + __uint_as_float(t[1]);
}
__device__ __forceinline__ float row2_sum(float x) {
  auto s = __builtin_amdgcn_permlane16_swap(__float_as_uint(x), __float_as_uint(x), false, false);
  return __uint_as_float(s[0]) + __uint_as_float(s[1]);
}
__device__ __forceinline__ float wave_sum(float x) {
  x += dppf<QP_XOR1>(x); x += dppf<QP_XOR2>(x); x += dppf<ROR4>(x); x += dppf<ROR8>(x);
  return xrow_sum(x);
}
template <int SRC, int DST> __device__ __forceinline__ unsigned bank_mov(unsigned old, unsigned x) {
  constexpr int sh = ((DST - SRC) & 3) * 4;
  constexpr int ctrl = sh == 0 ? QP_ID : (0x120 + sh);
  return (unsigned)__builtin_amdgcn_update_dpp((int)old, (int)x, ctrl, 0xf, 1 << DST, false);
}
__device__ __forceinline__ rsrc_t mkrs(const void* p, unsigned bytes) { return __builtin_amdgcn_make_buffer_rsrc((void*)p, 0, bytes, 0x00020000); }
__device__ __forceinline__ v2u trread(LAS unsigned char* p) {
  return __builtin_bit_cast(v2u, __builtin_amdgcn_ds_read_tr16_b64_v4i16((LAS s16x4*)p));
}
__device__ __forceinline__ v4u trfrag(LAS unsigned char* p, int rowstride4) {
  const v2u a = trread(p), b = trread(p + rowstride4);
  return v4u{a.x, a.y, b.x, b.y};
}
__device__ __forceinline__ v4u tagged(f32x4 v, v4u t) {
  return v4u{(__float_as_uint(v.x) & ~0x3ffu) | t.x, (__float_as_uint(v.y) & ~0x3ffu) | t.y, (__float_as_uint(v.z) & ~0x3ffu) | t.z, (__float_as_uint(v.w) & ~0x3ffu) | t.w};
}
__device__ __forceinline__ void untag(v4u& v, v4u t, unsigned& bad) { v ^= t; bad |= (v.x | v.y) | (v.z | v.w); }
__device__ __forceinline__ void lds_wait_ge(volatile LAS unsigned* p, unsigned v) {
  for (int s = 0; *p < v && s < LDS_SPIN_CAP; ++s) __builtin_amdgcn_s_sleep(1);
  CBAR();
}
__device__ __forceinline__ void lds_spin_ge(volatile LAS unsigned* p, unsigned v) {
  for (int s = 0; *p < v && s < LDS_SPIN_CAP; ++s) { }
  CBAR();
}
__device__ __forceinline__ void ln_row(const float (&v)[12], bool live, v2u (&out)[3]) {
  const float ref = __uint_as_float(__builtin_amdgcn_readfirstlane(__float_as_uint(v[0])));
  float s1 = 0.f, s2 = 0.f;
#pragma unroll
  for (int i = 0; i < 12; ++i) { const float d = live ? v[i] - ref : 0.f; s1 += d; s2 = fmaf(d, d, s2); }
  s1 = wave_sum(s1); s2 = wave_sum(s2);
  const float md = s1 * (1.f / 576.f);
  const float var = fmaxf(fmaf(-md, md, s2 * (1.f / 576.f)), 0.f);
  const float m = rbf(ref + md);
  const float den = rbf(__builtin_amdgcn_sqrtf(rbf(rbf(var) + 1e-5f)));
  const float inv = __builtin_amdgcn_rcpf(den);
#pragma unroll
  for (int i = 0; i < 3; ++i) {
    const unsigned a = cvtpk(v[4 * i] - m, v[4 * i + 1] - m), c = cvtpk(v[4 * i + 2] - m, v[4 * i + 3] - m);
    out[i] = v2u{cvtpk(bflo(a) * inv, bfhi(a) * inv), cvtpk(bflo(c) * inv, bfhi(c) * inv)};
  }
}

#define STAMP(i) do { } while (0)

__global__ void __launch_bounds__(512, 2) fused_block(const u16* __restrict__ x, const u16* __restrict__ Kc, const u16* __restrict__ Vc, const u16* __restrict__ wq,
    const u16* __restrict__ wo, const u16* __restrict__ wgt, const u16* __restrict__ wup, const u16* __restrict__ wdn, u16* out, unsigned char* ws) {
  __shared__ __attribute__((aligned(16))) unsigned char smem_[L_TOTAL];
  LAS unsigned char* L = (LAS unsigned char*)smem_;
  volatile LAS unsigned* FLG = (volatile LAS unsigned*)(L + L_FLG);
  const int tid = threadIdx.x, lane = tid & 63, w = __builtin_amdgcn_readfirstlane(tid >> 6);
#define MKDESC(p, bytes) v4u{(unsigned)(u64)(p), (unsigned)((u64)(p) >> 32) & 0xffffu, (unsigned)(bytes), 0x00020000u}
  const unsigned lbase = (unsigned)(size_t)L;
#define DMA_ASM(desc, ldsa, voff, soff, imm) asm volatile("s_mov_b32 m0, %0\n\ts_nop 0\n\tbuffer_load_dwordx4 %1, %2, %3 offen offset:%4 nt lds" \
    :: "s"((ldsa) - (imm)), "v"(voff), "s"(desc), "s"(soff), "i"(imm) : "memory")
  const bool live = lane < 48, two = (w == 2 || w == 7);
  const unsigned sw2 = (unsigned)((((lane & 7) ^ (((lane >> 4) & 1) << 1)) * 16) ^ (w << 6));
  v2u ep, xpk[3], xp2[3];
  if (w < 2) {
    {
      u64 pc; unsigned len;
      asm volatile("s_getpc_b64 %0\n.Lfb_code_begin:\n\ts_mov_b32 %1, .Lfb_code_end-.Lfb_code_begin" : "=s"(pc), "=s"(len));
      const v4u dc = MKDESC(pc, len);
      asm volatile("s_mov_b32 m0, %0\n\ts_nop 0\n\tbuffer_load_dword %1, %2, %3 offen lds\n\tbuffer_load_dword %1, %2, %4 offen lds"
        :: "s"(lbase + L_SCR + 2048), "v"(lane * 128), "s"(dc), "s"(w * 8192), "s"(w * 8192 + 16384) : "memory"); }
    const v4u dq = MKDESC(wq, 576 * 4096 * 2);
    const unsigned vo = (unsigned)((blockIdx.x & 3) * 144 + w * 8 + (lane >> 3)) * 8192u + (blockIdx.x >> 2) * 128 + sw2;
#pragma unroll
    for (int i = 0; i < 9; ++i) DMA_ASM(dq, lbase + L_WQ + w * 1024 + i * 2048, vo, i * 16 * 8192, 0);
    asm volatile("" : "=v"(ep), "=v"(xpk[0]), "=v"(xpk[1]), "=v"(xpk[2]), "=v"(xp2[0]), "=v"(xp2[1]), "=v"(xp2[2]));
  } else {
    const unsigned xo = (unsigned)(8 * (live ? lane : 47)), xo1 = xo + w * 1152, xo2 = xo + (w & 1) * 1152, z = 0;
    asm volatile("global_load_dwordx2 %0, %3, %4\n\tglobal_load_dwordx2 %1, %3, %4 offset:384\n\tglobal_load_dwordx2 %2, %3, %4 offset:768"
      : "=&v"(xpk[0]), "=&v"(xpk[1]), "=&v"(xpk[2]) : "v"(xo1), "s"(x) : "memory");
    asm volatile("global_load_dwordx2 %0, %1, %2 sc1" : "=&v"(ep) : "v"(z), "s"(ws) : "memory");
    if (two) asm volatile("global_load_dwordx2 %0, %3, %4\n\tglobal_load_dwordx2 %1, %3, %4 offset:384\n\tglobal_load_dwordx2 %2, %3, %4 offset:768"
      : "=&v"(xp2[0]), "=&v"(xp2[1]), "=&v"(xp2[2]) : "v"(xo2), "s"(x) : "memory");
    else asm volatile("" : "=v"(xp2[0]), "=v"(xp2[1]), "=v"(xp2[2]));
    if (w == 2) FLG[lane] = 0u;
  }
  WG_BARRIER();
  int wg = blockIdx.x;
  asm volatile("" : "+s"(wg));
  const int uu = MAPV == 0 ? wg : (MAPV == 1 ? (wg & 7) * 32 + (wg >> 3) : (MAPV == 2 ? ((wg * 37 + (wg >> 3) * 0) & 255) ^ ((wg >> 3) & 7) : (MAPV == 3 ? (wg ^ ((wg >> 3) & 7) ^ ((wg >> 6) & 3)) : (int)(__builtin_bitreverse32(wg) >> 24))));
  const int pair = uu >> 1, half = uu & 1, b = pair >> 4, kvh = pair & 15;
  const int rp = (pair & 7) * 16 + (pair >> 3);
  const int hsel = (pair >> 3) & 1;
  const bool holder = half == hsel;
  const bool isF = holder && rp < 72, isO = holder && rp >= 72 && rp < 108;
  const int fu = rp;
  const int sk = (rp - 72) & 3, sn = (rp - 72) >> 2;
  const int cb = wg >> 2, kq = wg & 3;
  const int n = lane & 15, g = lane >> 4, tq_ = (lane & 15) >> 2, tp = lane & 3;
  const int f2 = (tq_ >> 1) | ((g & 1) << 1);
  const rsrc_t rws = mkrs(ws, WS_END);
  const int dl = rp < 72 ? DELTA_F : (rp < 108 ? DELTA_O : 0);
  const int nt0 = 64 + (hsel ? dl : -dl);
  const int NT = half ? 128 - nt0 : nt0;
  const unsigned tbase = ((unsigned)pair << 20) + (half ? (unsigned)nt0 * 8192u : 0u);
  const int voffK = (n >> 1) * 256 + (2 * g + (n & 1)) * 16;
  const int voffV = g * 2048 + n * 16;
  v4u kr[8], vr[8];
#define KOFF(i) (voffK + (((i) >> 1) & 1) * 2048 + ((i) & 1) * 128)
#define LDK(i, so) kr[i] = __builtin_amdgcn_raw_buffer_load_b128(rk, KOFF(i), (so) + ((i) >> 2) * 4096, AUX_NT)
#define LDV(i, so) vr[i] = __builtin_amdgcn_raw_buffer_load_b128(rv, voffV + (i) * 256, (so), AUX_NT)
  const rsrc_t rk = mkrs(Kc, 128u << 20), rv = mkrs(Vc, 128u << 20);
#define DMA_PACE() asm volatile("s_waitcnt vmcnt(" STR(DMA_INFL) ")" ::: "memory")
#define LDS_INC(f) do { if (lane == 0) __hip_atomic_fetch_add((LAS unsigned*)(FLG + (f)), 1u, __ATOMIC_RELAXED, __HIP_MEMORY_SCOPE_WORKGROUP); } while (0)
  const rsrc_t rx = mkrs(x, 8 * 576 * 2);
  u64 tag = 0;
  const unsigned guvo = (unsigned)(lane >> 3) * 9216u + fu * 128 + (((lane & 7) ^ ((lane >> 4) & 1)) * 16);
#define GU_DMA(i) do { const int mt_ = (i) >= 72, ri_ = (i) - 72 * mt_; const v4u dg_ = MKDESC(mt_ ? wup : wgt, 576 * 4608 * 2); \
    DMA_ASM(dg_, lbase + (i) * 1024, guvo ^ (unsigned)(((ri_ & 1) << 5) | (mt_ << 6)), ri_ * 8 * 9216, 0); } while (0)
  const int tl0 = isF ? N_EARLY * 1024 : (isO ? 131072 : 0);
  const int dbk = w - 2;
#define DOWN_LOADS() do { const rsrc_t rd = mkrs(wdn, dsz); \
    const unsigned vo_ = (dbk < 4 || n < 8) ? (unsigned)(fu * 64 + 8 * g) * 1152u + dbk * 256 + n * 16 : OOB; \
    _Pragma("unroll") for (int i = 0; i < 16; ++i) df[i] = __builtin_amdgcn_raw_buffer_load_b128(rd, vo_, ((i >> 3) * 32 + (i & 7)) * 1152, AUX_NT); } while (0)
#define DOWN_TRANSPOSE() do { _Pragma("unroll") for (int ks = 0; ks < 2; ++ks) { v4u t_[8]; \
      _Pragma("unroll") for (int jj = 0; jj < 8; ++jj) _Pragma("unroll") for (int mm = 0; mm < 4; ++mm) \
        t_[jj][mm] = __builtin_amdgcn_perm(df[ks * 8 + 2 * mm + 1][jj >> 1], df[ks * 8 + 2 * mm][jj >> 1], (jj & 1) ? 0x07060302u : 0x05040100u); \
      _Pragma("unroll") for (int jj = 0; jj < 8; ++jj) { df[ks * 8 + jj] = t_[jj]; asm volatile("" : "+v"(df[ks * 8 + jj])); } } } while (0)

  const int nrounds = max((147456 - 4096 - tl0) >> 15, 0);
  if (w < 2) {
    static_assert(DMA_INFL == 12, "the wq signal sits behind the 12th piece");
    const int nw = isF ? N_EARLY : (isO ? 128 : 0);
#pragma unroll 1
    for (int run = 0; run < 2; ++run) {
      const int p0 = isF ? 72 * run : 64 * run, p1 = min(nw, isF ? p0 + 72 : p0 + 64);
      if (p1 <= p0) break;
      const v4u dw = isF ? MKDESC(run ? wup : wgt, 576 * 4608 * 2) : MKDESC(wo, 4096 * 576 * 2);
      const unsigned vo = isF ? (guvo ^ (unsigned)((w << 5) | (run << 6))) : (unsigned)(sk * 1024 + (lane >> 3)) * 1152u + sn * 128 + sw2;
      const unsigned sstep = isF ? 16u * 9216u : 16u * 1152u;
      unsigned so = (isF ? 0u : (unsigned)p0 * 8u * 1152u) + (sstep >> 1) * w, la = lbase + (p0 + w) * 1024;
      if (run) lds_wait_ge(FLG + F_QDONE, 4u);
#pragma unroll 1
      for (int i = p0; i < p1; i += 8) {
#pragma unroll
        for (int j = 0; j < 4; ++j) { DMA_ASM(dw, la, vo, so, 0); DMA_PACE(); la += 2048; so += sstep; }
        if (i == 16) LDS_INC(F_XN);
      }
    }
    const v4u dk = MKDESC(Kc, 128u << 20), dv = MKDESC(Vc, 128u << 20);
#pragma unroll 1
    for (int rd = 0; rd < nrounds; ++rd) {
      const int slot = 2 * rd + w;
      if (tl0 + 4096 + (slot + 1) * 16384 > L_WQ) lds_wait_ge(FLG + F_QDONE, 4u);
      const unsigned so = tbase + (unsigned)slot * 8192u, la = lbase + tl0 + 4096 + slot * 16384;
#pragma unroll
      for (int i = 0; i < 8; ++i) { DMA_ASM(dk, la + i * 1024, voffK, so + (i >> 2) * 4096, ((i >> 1) & 1) * 2048 + (i & 1) * 128); DMA_PACE(); }
#pragma unroll
      for (int i = 0; i < 8; ++i) { DMA_ASM(dv, la + 8192 + i * 1024, voffV, so, i * 256); DMA_PACE();
        if (i == 3 && rd == 0 && nw == 0) LDS_INC(F_XN); }
      FLG[F_FULL + w] = rd;
    }
    STAMP(3);
    SB();
#pragma unroll
    for (int i = 0; i < 8; ++i) { LDK(i, tbase + (2 * nrounds + w) * 8192); SB(); DMA_PACE(); }
#pragma unroll
    for (int i = 0; i < 8; ++i) { LDV(i, tbase + (2 * nrounds + w) * 8192); SB(); DMA_PACE(); }
    FLG[F_FULL + w] = nrounds;
  } else {
    if (two) asm volatile("s_waitcnt vmcnt(4)" ::: "memory"); else asm volatile("s_waitcnt vmcnt(1)" ::: "memory");
    asm volatile("" : "+v"(xpk[0]), "+v"(xpk[1]), "+v"(xpk[2]));
#define LN_TO_XN(src, row) do { float v[12]; \
      _Pragma("unroll") for (int i = 0; i < 3; ++i) { v[4 * i] = bflo(src[i].x); v[4 * i + 1] = bfhi(src[i].x); v[4 * i + 2] = bflo(src[i].y); v[4 * i + 3] = bfhi(src[i].y); } \
      v2u o3[3]; ln_row(v, live, o3); \
      if (live) { _Pragma("unroll") for (int i = 0; i < 3; ++i) *(LAS v2u*)(L + L_XN + (row) * XN_STR + (192 * i + 4 * lane) * 2) = o3[i]; } } while (0)
    LN_TO_XN(xpk, w);
    SB();
    asm volatile("s_waitcnt vmcnt(0)" : "+v"(ep), "+v"(xp2[0]), "+v"(xp2[1]), "+v"(xp2[2]) :: "memory");
    if (two) LN_TO_XN(xp2, w & 1);
    asm volatile("s_waitcnt lgkmcnt(0)" ::: "memory");
    LDS_INC(F_XN);
    tag = ((u64)(unsigned)__builtin_amdgcn_readfirstlane(ep.y) << 32 | (unsigned)__builtin_amdgcn_readfirstlane(ep.x)) + TAG_STEP;
    const v4u tg = TAG_FIELDS(tag);
    if (w == 7) { FLG[F_TAG] = (unsigned)tag; FLG[F_TAG + 1] = (unsigned)(tag >> 32); }
    const v4u tq4 = tg & 15u;
    if (!two) {
      lds_spin_ge(FLG + F_XN, 8u);
      const int tq = w - 3;
      v4u a[5], bb[5];
#pragma unroll
      for (int ks = 0; ks < 5; ++ks) {
        const int gg = ks < 4 ? g : (g & 1);
        a[ks] = trfrag(L + L_WQ + (32 * ks + 8 * gg + tq_) * 128 + ((tq ^ f2) * 32) + tp * 8, 4 * 128);
        bb[ks] = *(LAS v4u*)(L + L_XN + (n & 7) * XN_STR + (kq * 144 + 32 * ks + 8 * gg) * 2);
      }
      SB();
      if (g >= 2) bb[4] = v4u{0, 0, 0, 0};
      f32x4 d = {0.f, 0.f, 0.f, 0.f};
#pragma unroll
      for (int ks = 0; ks < 5; ++ks) d = mfma32(a[ks], bb[ks], d);
      __builtin_amdgcn_raw_buffer_store_b128((__builtin_bit_cast(v4u, d) & ~15u) | tq4, rws, n < 8 ? WS_QG + ((n * 4 + kq) * 4096 + cb * 64 + tq * 16 + 4 * g) * 4 : OOB, 0, AUX_SC1);
      LDS_INC(F_QDONE);
      STAMP(2);
    }
    if (w == 7) {
      const int qo = WS_QG + (b * 4 * 4096 + kvh * 256 + 4 * lane) * 4;
      v4u a[4];
      if (QPOLL_DELAY) __builtin_amdgcn_s_sleep(QPOLL_DELAY);
#pragma unroll 1
      for (int s = 0; s < SPIN_CAP; ++s) {
#pragma unroll
        for (int i = 0; i < 4; ++i) a[i] = __builtin_amdgcn_raw_buffer_load_b128(rws, qo, i * 16384, QPOLL_AUX);
        unsigned bad = 0;
#pragma unroll
        for (int i = 0; i < 4; ++i) untag(a[i], tq4, bad);
        bad |= dppu<QP_XOR1>(bad); bad |= dppu<QP_XOR2>(bad);
        if ((bad & 15u) == 0u) break;
        __builtin_amdgcn_s_sleep(2);
        CBAR();
      }
#define QV(p) __uint_as_float((p) | 8u)
#define QS(c) ((QV(a[0].c) + QV(a[1].c)) + (QV(a[2].c) + QV(a[3].c)))
      *(LAS v2u*)(L + L_Q + lane * 8) = v2u{cvtpk(QS(x), QS(y)), cvtpk(QS(z), QS(w))};
      asm volatile("s_waitcnt lgkmcnt(0)" ::: "memory");
      FLG[F_QREADY] = 1u;
      STAMP(2);
      {
        unsigned j0 = __builtin_amdgcn_raw_buffer_load_b32(rws, lane < 3 ? WS_O + lane * (768 << 10) : OOB, 0, AUX_NT);
        unsigned j1 = __builtin_amdgcn_raw_buffer_load_b32(mkrs(out, 8 * 576 * 2), lane == 0 ? 0 : OOB, 0, AUX_NT);
        unsigned j2 = __builtin_amdgcn_raw_buffer_load_b32(mkrs(wdn, 4608 * 576 * 2), (isF && lane < 2) ? (fu * 64 + lane * 63) * 1152 : OOB, 0, AUX_NT);
        asm volatile("" :: "v"(j0), "v"(j1), "v"(j2)); }
    }
#pragma unroll
    for (int i = 0; i < 8; ++i) asm volatile("" : "=v"(kr[i]), "=v"(vr[i]));
  }
  if (w != 7) {
    lds_wait_ge(FLG + F_QREADY, 1u);
    STAMP(4);
    v4u qf[8];
    { const v4u qa0 = *(LAS v4u*)(L + L_Q + (((n >> 1) & 1) * 128 + 16 * g + 8 * (n & 1)) * 2), qa1 = *(LAS v4u*)(L + L_Q + (((n >> 1) & 1) * 128 + 64 + 16 * g + 8 * (n & 1)) * 2);
#pragma unroll
      for (int i = 0; i < 8; ++i) qf[i] = ((n >> 2) == (i >> 1)) ? ((i & 1) ? qa1 : qa0) : v4u{0, 0, 0, 0}; }
    const bool par = lane & 1;
    const unsigned jbmask = par ? 0xffff0000u : 0x0000ffffu;
    float m = -INFINITY, l = 0.f;
    f32x4 o = {0.f, 0.f, 0.f, 0.f};
    v4u pa0 = {0, 0, 0, 0}, pa1 = {0, 0, 0, 0};
#define PV(i, pa, xs_, MORE) { pa.x = bank_mov<((i) >> 1), 0>(pa.x, xs_); pa.y = bank_mov<((i) >> 1), 1>(pa.y, xs_); pa.z = bank_mov<((i) >> 1), 2>(pa.z, xs_); pa.w = bank_mov<((i) >> 1), 3>(pa.w, xs_); \
        o = mfma32(pa, vr[i], o); if (MORE) { WIN(); LDV(i, so); } SB(); }
#define BODY(MORE) { \
      f32x4 d = {0.f, 0.f, 0.f, 0.f}; \
      _Pragma("unroll") for (int i = 0; i < 8; ++i) { d = mfma32(qf[i], kr[i], d); if (MORE) { WIN(); LDK(i, so); } SB(); } \
      const float keep = par ? d.w : d.x, send = par ? d.y : d.z; \
      float s = keep + dppf<QP_XOR1>(send); \
      s = rbf(rbf(s) * 0.08838834764831845f); \
      if (__any(s > m)) { \
        float t = s; t = fmaxf(t, dppf<QP_XOR2>(t)); t = fmaxf(t, dppf<ROR4>(t)); t = fmaxf(t, dppf<ROR8>(t)); t = xrow_max(t); \
        const float mn = fmaxf(m, t); \
        const float al = __builtin_amdgcn_exp2f((m - mn) * LOG2E); \
        m = mn; l *= al; \
        const float a0 = dppf<QP_0000>(al), a1 = dppf<QP_1111>(al); \
        o.x *= a0; o.y *= a0; o.z *= a1; o.w *= a1; \
      } \
      const float p = __builtin_amdgcn_exp2f((s - m) * LOG2E); \
      l += p; \
      const unsigned p2 = cvtpk(p, p); \
      const unsigned x0 = dppu<QP_0011>(p2) & jbmask, x1 = dppu<QP_2233>(p2) & jbmask; \
      PV(0, pa0, x0, MORE) PV(1, pa1, x1, MORE) PV(2, pa0, x0, MORE) PV(3, pa1, x1, MORE) PV(4, pa0, x0, MORE) PV(5, pa1, x1, MORE) PV(6, pa0, x0, MORE) PV(7, pa1, x1, MORE) }
    if (w < 2) {
#pragma unroll 1
      for (int tnext = 2 * nrounds + 2 + w; tnext < NT; tnext += 2) { const unsigned so = tbase + (unsigned)tnext * 8192u;
        BODY(true)
      }
    }
#define REC_O(r) (*(LAS f32x4*)(L + L_REC + (r) * REC_STR + lane * 16))
#define REC_ML(r) (*(LAS f32x2_t*)(L + L_REC + (r) * REC_STR + 1024 + lane * 8))
#define MERGE(ro, rml) do { const float mn_ = fmaxf(m, rml.x), ms_ = mn_ == -INFINITY ? 0.f : mn_; \
      const float ea_ = __builtin_amdgcn_exp2f((m - ms_) * LOG2E), eb_ = __builtin_amdgcn_exp2f((rml.x - ms_) * LOG2E); \
      m = mn_; l = __fadd_rn(__fmul_rn(l, ea_), __fmul_rn(rml.y, eb_)); \
      const float a0_ = dppf<QP_0000>(ea_), a1_ = dppf<QP_1111>(ea_), b0_ = dppf<QP_0000>(eb_), b1_ = dppf<QP_1111>(eb_); \
      o.x = __fadd_rn(__fmul_rn(o.x, a0_), __fmul_rn(ro.x, b0_)); o.y = __fadd_rn(__fmul_rn(o.y, a0_), __fmul_rn(ro.y, b0_)); \
      o.z = __fadd_rn(__fmul_rn(o.z, a1_), __fmul_rn(ro.z, b1_)); o.w = __fadd_rn(__fmul_rn(o.w, a1_), __fmul_rn(ro.w, b1_)); } while (0)
    if (w == 0 && nrounds > 0) {
      lds_wait_ge(FLG + F_CHAIN, 5u);
      const f32x4 ro = REC_O(2); const f32x2_t rml = REC_ML(2); MERGE(ro, rml);
    }
    const u64 tagp = (u64)(unsigned)__builtin_amdgcn_readfirstlane(FLG[F_TAG + 1]) << 32 | (unsigned)__builtin_amdgcn_readfirstlane(FLG[F_TAG]);
#pragma unroll 1
    for (int t = w < 2 ? -1 : w - 2; t < 2 * nrounds; t += 5) {
      if (t >= 0) {
        lds_wait_ge(FLG + F_FULL + (t & 1), (unsigned)((t >> 1) + 1));
#pragma unroll
        for (int i = 0; i < 8; ++i) { kr[i] = *(LAS v4u*)(L + tl0 + 4096 + t * 16384 + i * 1024 + lane * 16); vr[i] = *(LAS v4u*)(L + tl0 + 4096 + t * 16384 + 8192 + i * 1024 + lane * 16); }
      }
      const unsigned so = 0; BODY(false)
      if (t < 0) break;
    }
    STAMP(5);
    bool pub = true;
    if (w >= 2) {
      if (w == 2) lds_wait_ge(FLG + F_QDONE, 4u);
      else lds_wait_ge(FLG + F_CHAIN, (unsigned)(w - 2));
    } else {
      if (FLG[F_TICK] == 0u) {
        REC_O(w) = o; REC_ML(w) = f32x2_t{m, l};
        asm volatile("s_waitcnt lgkmcnt(0)" ::: "memory");
        unsigned tk = 0;
        if (lane == 0) tk = __hip_atomic_fetch_add((LAS unsigned*)(FLG + F_TICK), 1u, __ATOMIC_RELAXED, __HIP_MEMORY_SCOPE_WORKGROUP);
        pub = __builtin_amdgcn_readfirstlane(tk) != 0u;
      }
      CBAR();
    }
    if (pub) {
      if (w != 2) { const int r = w < 2 ? 1 - w : 2; const f32x4 ro = REC_O(r); const f32x2_t rml = REC_ML(r); MERGE(ro, rml); }
      if (w >= 2) { REC_O(2) = o; REC_ML(2) = f32x2_t{m, l}; asm volatile("s_waitcnt lgkmcnt(0)" ::: "memory"); FLG[F_CHAIN] = w - 1; }
      const rsrc_t rp_ = mkrs(ws, w < 2 ? WS_END : 0);
      const v4u tg = TAG_FIELDS(tagp);
      l += dppf<QP_XOR2>(l); l += dppf<ROR4>(l); l += dppf<ROR8>(l); l = xrow_sum(l);
      const float inv = __builtin_amdgcn_rcpf(l), i0 = dppf<QP_0000>(inv), i1 = dppf<QP_1111>(inv);
      const int slot = kvh * 16 + b * 2 + half;
      __builtin_amdgcn_raw_buffer_store_b128(tagged(f32x4{o.x * i0, o.y * i0, o.z * i1, o.w * i1}, tg), rp_, WS_O + slot * 1024 + (n >> 2) * 256 + g * 64 + (n & 3) * 16, 0, AUX_SC1);
      const float lse = fmaf(m, LOG2E, __builtin_amdgcn_logf(l)), lse1 = dppf<QP_1111>(lse);
      __builtin_amdgcn_raw_buffer_store_b128(tagged(f32x4{lse, __uint_as_float(__float_as_uint(lse) << 22), lse1, __uint_as_float(__float_as_uint(lse1) << 22)}, tg), rp_, lane == 0 ? WS_LSE + slot * 16 : OOB, 0, AUX_SC1);
      __builtin_amdgcn_raw_buffer_store_b32((unsigned)(tagp >> 32), rp_, lane == 0 ? WS_DONE + wg * 4 : OOB, 0, AUX_SC1);
    }
  }
#pragma unroll 1
  for (int real = (w == 7) ? 0 : 1; real < 2; ++real) {
  const rsrc_t rt = mkrs(ws, real ? WS_END : 0);
  int lane_ = tid & 63;
  asm volatile("" : "+v"(lane_));
  const int lane = lane_, n = lane & 15, g = lane >> 4, tq_ = (lane & 15) >> 2, tp = lane & 3, f2 = (tq_ >> 1) | ((g & 1) << 1);
  const bool live = lane < 48;
  if (real) WG_BARRIER();
  STAMP(7);
  tag = real ? ((u64)(unsigned)__builtin_amdgcn_readfirstlane(FLG[F_TAG + 1]) << 32 | (unsigned)__builtin_amdgcn_readfirstlane(FLG[F_TAG])) : 0ull;
  const v4u tg = TAG_FIELDS(tag);
  const unsigned thint = (unsigned)(tag >> 32);
  if (isO) {
    v4u af[16];
#pragma unroll
    for (int i = 0; i < 16; ++i) af[i] = trfrag(L + L_WO + ((w >> 1) * 256 + (i >> 3) * 128 + (w & 1) * 64 + ((i >> 2) & 1) * 32 + 8 * g + tq_) * 128 + (((i & 3) ^ f2) * 32) + tp * 8, 4 * 128);
    const int so_ = (sk * 4 + (w >> 1)) * 16 + n;
    v4u of[8], ls;
#pragma unroll 1
    for (int s = 0; s < SPIN_CAP; ++s) {
      ls = __builtin_amdgcn_raw_buffer_load_b128(rt, WS_LSE + so_ * 16, 0, AUX_SC1);
#pragma unroll
      for (int i = 0; i < 8; ++i) of[i] = __builtin_amdgcn_raw_buffer_load_b128(rt, WS_O + so_ * 1024 + (w & 1) * 512 + g * 16 + i * 64, 0, AUX_SC1);
      unsigned bad = 0;
      untag(ls, tg, bad);
#pragma unroll
      for (int i = 0; i < 8; ++i) untag(of[i], tg, bad);
      if ((bad & 0x3ffu) == 0u) break;
      __builtin_amdgcn_s_sleep(1);
      CBAR();
    }
    STAMP(9);
#define U2F __uint_as_float
    f32x4 d[8];
#pragma unroll
    for (int t = 0; t < 8; ++t) d[t] = f32x4{0.f, 0.f, 0.f, 0.f};
#pragma unroll
    for (int ks = 0; ks < 2; ++ks) {
      const v4u b0 = {cvtpk(U2F(of[4 * ks].x), U2F(of[4 * ks].y)), cvtpk(U2F(of[4 * ks + 1].x), U2F(of[4 * ks + 1].y)), cvtpk(U2F(of[4 * ks + 2].x), U2F(of[4 * ks + 2].y)), cvtpk(U2F(of[4 * ks + 3].x), U2F(of[4 * ks + 3].y))};
      const v4u b1 = {cvtpk(U2F(of[4 * ks].z), U2F(of[4 * ks].w)), cvtpk(U2F(of[4 * ks + 1].z), U2F(of[4 * ks + 1].w)), cvtpk(U2F(of[4 * ks + 2].z), U2F(of[4 * ks + 2].w)), cvtpk(U2F(of[4 * ks + 3].z), U2F(of[4 * ks + 3].w))};
#pragma unroll
      for (int t = 0; t < 4; ++t) { d[t] = mfma32(af[ks * 4 + t], b0, d[t]); d[4 + t] = mfma32(af[8 + ks * 4 + t], b1, d[4 + t]); }
    }
    const float e0 = U2F(ls.x | (ls.y >> 22)), e1 = U2F(ls.z | (ls.w >> 22));
    const float w0 = __builtin_amdgcn_rcpf(1.f + __builtin_amdgcn_exp2f(dppf<QP_XOR1>(e0) - e0)), w1 = __builtin_amdgcn_rcpf(1.f + __builtin_amdgcn_exp2f(dppf<QP_XOR1>(e1) - e1));
    {
      const bool od = n & 1;
      f32x4 r[4];
#pragma unroll
      for (int t = 0; t < 4; ++t) r[t] = w0 * d[t] + w1 * d[4 + t];
      f32x4 k0 = od ? r[2] : r[0], k1 = od ? r[3] : r[1], s0 = od ? r[0] : r[2], s1 = od ? r[1] : r[3];
      k0.x += dppf<QP_XOR1>(s0.x); k0.y += dppf<QP_XOR1>(s0.y); k0.z += dppf<QP_XOR1>(s0.z); k0.w += dppf<QP_XOR1>(s0.w);
      k1.x += dppf<QP_XOR1>(s1.x); k1.y += dppf<QP_XOR1>(s1.y); k1.z += dppf<QP_XOR1>(s1.z); k1.w += dppf<QP_XOR1>(s1.w);
      LAS unsigned char* dst = L + (real ? L_RED1 + w * 2048 : L_SCR) + (n >> 1) * 256 + (od ? 128 : 0) + g * 16;
      *(LAS f32x4*)dst = k0; *(LAS f32x4*)(dst + 64) = k1; }
    if (real) WG_BARRIER();
    {
      f32x4 t = *(LAS f32x4*)(L + L_RED1 + g * 4096 + w * 256 + n * 16) + *(LAS f32x4*)(L + L_RED1 + g * 4096 + 2048 + w * 256 + n * 16);
      t.x = xrow_sum(t.x); t.y = xrow_sum(t.y); t.z = xrow_sum(t.z); t.w = xrow_sum(t.w);
      __builtin_amdgcn_raw_buffer_store_b128(tagged(t, tg), rt, lane < 16 ? WS_H + ((w * 4 + sk) * 576 + sn * 64 + n * 4) * 4 : OOB, 0, AUX_SC1);
      STAMP(10);
    }
  } else if (isF) {
    v4u df[16];
    const bool dwave = (w >= 2 && w < 7) || !real;
    if (N_EARLY < 144 && real) {
#pragma unroll 1
      for (int i = N_EARLY + w; i < 144; i += 8) GU_DMA(i);
    }
    if (dwave) { const unsigned dsz = real ? 4608 * 576 * 2 : 0; DOWN_LOADS(); DOWN_TRANSPOSE(); }
    else {
#pragma unroll
      for (int i = 0; i < 16; ++i) asm volatile("" : "=v"(df[i]));
    }
    if (N_EARLY < 144 && real) { asm volatile("s_waitcnt vmcnt(0)" ::: "memory"); WG_BARRIER(); }
    STAMP(6);
    SB();
    v2u xpk[3];
#pragma unroll
    for (int i = 0; i < 3; ++i) xpk[i] = __builtin_amdgcn_raw_buffer_load_b64(rx, 8 * (live ? lane : 47) + 384 * i, w * 1152, 0);
    v4u gf[18];
    { LAS unsigned char* ga = L + ((tp & 1) ? L_UP : L_GATE) + (8 * g + tq_) * 128 + ((w ^ (f2 | ((tp & 1) << 2))) * 16) + (tp >> 1) * 8;
#pragma unroll
      for (int ks = 0; ks < 18; ++ks) gf[ks] = trfrag(ga + ks * 32 * 128, 4 * 128); }
#pragma unroll
    for (int ks = 0; ks < 18; ++ks) asm volatile("" : "+v"(gf[ks]));
    asm volatile("" : "+v"(xpk[0]), "+v"(xpk[1]), "+v"(xpk[2]));
    if (w == 7) {
#pragma unroll 1
      for (int s = 0; s < SPIN_CAP; ++s) {
        const v4u f0 = __builtin_amdgcn_raw_buffer_load_b128(rt, WS_DONE + lane * 16, 0, AUX_SC1);
        if (((f0.x ^ thint) | (f0.y ^ thint) | (f0.z ^ thint) | (f0.w ^ thint)) == 0u) break;
        __builtin_amdgcn_s_sleep(8);
        CBAR();
      }
    }
    if (real) WG_BARRIER();
    int hoff[3];
#pragma unroll
    for (int i = 0; i < 3; ++i) { const int c = 24 * i + (lane >> 1); hoff[i] = (c & 3) * 288 + (c >> 2) * 16 + (lane & 1) * 8; asm volatile("" : "+v"(hoff[i])); }
    v4u hp[12];
#pragma unroll 1
    for (int s = 0; s < SPIN_CAP; ++s) {
#pragma unroll
      for (int i = 0; i < 12; ++i) hp[i] = __builtin_amdgcn_raw_buffer_load_b128(rt, WS_H + (w * 4 * 576 + 4 * (live ? lane : 47)) * 4, i * 768, AUX_SC1);
      unsigned bad = 0;
#pragma unroll
      for (int i = 0; i < 12; ++i) untag(hp[i], tg, bad);
      if ((bad & 0x3ffu) == 0u) break;
      CBAR();
    }
    STAMP(9);
    { float v[12];
#pragma unroll
      for (int i = 0; i < 3; ++i) {
        const f32x4 a = (__builtin_bit_cast(f32x4, hp[i]) + __builtin_bit_cast(f32x4, hp[3 + i])) + (__builtin_bit_cast(f32x4, hp[6 + i]) + __builtin_bit_cast(f32x4, hp[9 + i]));
        const unsigned r0 = cvtpk(a.x, a.y), r1 = cvtpk(a.z, a.w);
        const unsigned h0 = cvtpk(bflo(xpk[i].x) + bflo(r0), bfhi(xpk[i].x) + bfhi(r0)), h1 = cvtpk(bflo(xpk[i].y) + bflo(r1), bfhi(xpk[i].y) + bfhi(r1));
        v[4 * i] = bflo(h0); v[4 * i + 1] = bfhi(h0); v[4 * i + 2] = bflo(h1); v[4 * i + 3] = bfhi(h1); }
      v2u o3[3]; ln_row(v, live, o3);
      if (live) {
#pragma unroll
        for (int i = 0; i < 3; ++i) *(LAS v2u*)(L + (real ? L_XN + w * HN_STR : L_SCR) + hoff[i]) = o3[i]; } }
    if (real) WG_BARRIER();
    STAMP(11);
    {
      v4u hb[9];
#pragma unroll
      for (int ks = 0; ks < 9; ++ks) hb[ks] = *(LAS v4u*)(L + L_XN + (n & 7) * HN_STR + g * 288 + (n >> 3) * 144 + ks * 16);
      SB();
      f32x4 d = {0.f, 0.f, 0.f, 0.f}, d2 = {0.f, 0.f, 0.f, 0.f};
#pragma unroll
      for (int ks = 0; ks < 9; ++ks) { d = mfma32(gf[ks], hb[ks], d); d2 = mfma32(gf[9 + ks], hb[ks], d2); }
      d.x += dppf<ROR8>(d2.x); d.y += dppf<ROR8>(d2.y); d.z += dppf<ROR8>(d2.z); d.w += dppf<ROR8>(d2.w);
      auto s0 = __builtin_amdgcn_permlane16_swap(__float_as_uint(d.x), __float_as_uint(d.z), false, false);
      auto s1 = __builtin_amdgcn_permlane16_swap(__float_as_uint(d.y), __float_as_uint(d.w), false, false);
      float av[2];
#pragma unroll
      for (int e = 0; e < 2; ++e) {
        const float gg = rbf(__uint_as_float(e ? s1[0] : s0[0])), uu = rbf(__uint_as_float(e ? s1[1] : s0[1]));
        const float z2 = gg * fmaf(gg * gg, 0.044715f, 1.f) * (2.f * 0.7978845608028654f * LOG2E);
        const float ge = rbf(gg * __builtin_amdgcn_rcpf(1.f + __builtin_amdgcn_exp2f(-z2)));
        av[e] = ge * uu; }
      unsigned pk = cvtpk(av[0], av[1]);
      asm volatile("" : "+v"(pk));
      if (n < 8) *(LAS unsigned*)(L + L_ACT + n * ACT_STR + (8 * w + 2 * g) * 2) = pk; }
    if (real) WG_BARRIER();
    STAMP(12);
    if (dwave) {
      const v4u b0 = *(LAS v4u*)(L + L_ACT + (n & 7) * ACT_STR + g * 16), b1 = *(LAS v4u*)(L + L_ACT + (n & 7) * ACT_STR + 64 + g * 16);
      f32x4 d[8];
#pragma unroll
      for (int jj = 0; jj < 8; ++jj) d[jj] = mfma32(df[jj], b0, f32x4{0.f, 0.f, 0.f, 0.f});
#pragma unroll
      for (int jj = 0; jj < 8; ++jj) d[jj] = mfma32(df[8 + jj], b1, d[jj]);
      const bool hq = n >= 8;
      const unsigned po = (dbk < 4 || g < 2) ? WS_P + ((32 * dbk + 8 * g + (n >> 3)) * 72 + fu) * 128 + (n & 7) * 16 : OOB;
#pragma unroll
      for (int e = 0; e < 4; ++e)
        __builtin_amdgcn_raw_buffer_store_b128(tagged(f32x4{hq ? d[4][e] : d[0][e], hq ? d[5][e] : d[1][e], hq ? d[6][e] : d[2][e], hq ? d[7][e] : d[3][e]}, tg), rt, po, 2 * e * 72 * 128, AUX_SC1);
    }
  } else {
    const int unit = rp < 108 ? rp : 108 + (rp - 108) * 2 + half;
    if (unit < 144) {
      const int ob_ = lane & 7, oc = unit * 4;
      v2u xo = {0, 0};
      f32x4 a = {0.f, 0.f, 0.f, 0.f};
      if (w == 7) {
        xo = *(const GAS v2u*)(x + ob_ * 576 + oc);
        v4u hv;
#pragma unroll 1
        for (int s = 0; s < SPIN_CAP; ++s) {
          hv = __builtin_amdgcn_raw_buffer_load_b128(rt, WS_H + ((ob_ * 4 + ((lane >> 3) & 3)) * 576 + oc) * 4, 0, AUX_SC1);
          unsigned bad = 0; untag(hv, tg, bad);
          if ((bad & 0x3ffu) == 0u) break;
          __builtin_amdgcn_s_sleep(16);
          CBAR();
        }
        a = __builtin_bit_cast(f32x4, hv);
        a.x += dppf<ROR8>(a.x); a.y += dppf<ROR8>(a.y); a.z += dppf<ROR8>(a.z); a.w += dppf<ROR8>(a.w);
        a.x = row2_sum(a.x); a.y = row2_sum(a.y); a.z = row2_sum(a.z); a.w = row2_sum(a.w);
      }
      if (real) WG_BARRIER();
      STAMP(9);
      v4u p0, p1;
#pragma unroll 1
      for (int s = 0; s < SPIN_CAP; ++s) {
        p0 = __builtin_amdgcn_raw_buffer_load_b128(rt, WS_P + unit * 9216 + w * 1024 + lane * 16, 0, PPOLL_AUX);
        p1 = __builtin_amdgcn_raw_buffer_load_b128(rt, WS_P + unit * 9216 + 8192 + w * 128 + (lane & 7) * 16, 0, PPOLL_AUX);
        unsigned bad = 0; untag(p0, tg, bad); untag(p1, tg, bad);
        if ((bad & 0x3ffu) == 0u) break;
        __builtin_amdgcn_s_sleep(1);
        CBAR();
      }
      STAMP(11);
      f32x4 t = __builtin_bit_cast(f32x4, p0);
      t.x += dppf<ROR8>(t.x); t.y += dppf<ROR8>(t.y); t.z += dppf<ROR8>(t.z); t.w += dppf<ROR8>(t.w);
      t.x = xrow_sum(t.x); t.y = xrow_sum(t.y); t.z = xrow_sum(t.z); t.w = xrow_sum(t.w);
      if (lane < 8) *(LAS f32x4*)(L + L_RED + w * 128 + lane * 16) = t + __builtin_bit_cast(f32x4, p1);
      if (real) WG_BARRIER();
      if (w == 7 && lane < 8) {
        f32x4 f = *(LAS f32x4*)(L + L_RED + lane * 16);
#pragma unroll
        for (int r = 1; r < 8; ++r) f += *(LAS f32x4*)(L + L_RED + r * 128 + lane * 16);
        const unsigned r0 = cvtpk(a.x, a.y), r1 = cvtpk(a.z, a.w);
        const unsigned hh0 = cvtpk(bflo(xo.x) + bflo(r0), bfhi(xo.x) + bfhi(r0)), hh1 = cvtpk(bflo(xo.y) + bflo(r1), bfhi(xo.y) + bfhi(r1));
        const unsigned f0 = cvtpk(f.x, f.y), f1 = cvtpk(f.z, f.w);
        __builtin_amdgcn_raw_buffer_store_b64(v2u{cvtpk(bflo(hh0) + bflo(f0), bfhi(hh0) + bfhi(f0)), cvtpk(bflo(hh1) + bflo(f1), bfhi(hh1) + bfhi(f1))}, mkrs(out, real ? 8 * 576 * 2 : 0), (ob_ * 576 + oc) * 2, 0, 0);
        if (unit == 0 && lane == 0) __builtin_amdgcn_raw_buffer_store_b64(v2u{(unsigned)tag, (unsigned)(tag >> 32)}, rt, WS_EPOCH, 0, AUX_SC1);
        STAMP(15);
      }
    }
  }
  }
  asm volatile(".Lfb_code_end:");
}

extern "C" void kernel_launch(void* const* d_in, const int* in_sizes, int n_in, void* d_out, int out_size, void* d_ws, size_t ws_size, hipStream_t stream) {
  fused_block<<<256, 512, 0, stream>>>((const u16*)d_in[0], (const u16*)d_in[1], (const u16*)d_in[2], (const u16*)d_in[3], (const u16*)d_in[4],
      (const u16*)d_in[6], (const u16*)d_in[7], (const u16*)d_in[8], (u16*)d_out, (unsigned char*)d_ws);
}
```

```cpp
#include <hip/hip_runtime.h>
#include <hip/hip_bf16.h>

typedef unsigned short u16;
typedef unsigned long long u64;
typedef unsigned v4u __attribute__((ext_vector_type(4)));
typedef unsigned v2u __attribute__((ext_vector_type(2)));
typedef float f32x4 __attribute__((ext_vector_type(4)));
typedef short bf16x8 __attribute__((ext_vector_type(8)));
typedef short s16x4 __attribute__((ext_vector_type(4)));
typedef __bf16 bf16x2_t __attribute__((ext_vector_type(2)));
typedef float f32x2_t __attribute__((ext_vector_type(2)));
#define LAS __attribute__((address_space(3)))
#define GAS __attribute__((address_space(1)))
typedef __amdgpu_buffer_rsrc_t rsrc_t;

#define STAMPS 0
#define DELTA_F 4
#define DELTA_O 4
#define N_EARLY 144
#define DOWN_EARLY 0

constexpr int WS_EPOCH = 0;
constexpr int WS_DONE = 4096;
constexpr int WS_LSE = 8192;
constexpr int WS_H = 16384;
constexpr int WS_QG = 131072;
constexpr int WS_O = WS_QG + 8 * 4 * 4096 * 4;
constexpr int WS_P = WS_O + 32 * 16 * 512;
constexpr int WS_END = WS_P + 144 * 72 * 128;
constexpr int WS_DBG = 4 << 20;
constexpr int L_GATE = 0, L_UP = 73728, L_WO = 0, L_RED1 = 131072, L_WQ = 129024;
constexpr int L_XN = 147456, XN_STR = 1168, HN_STR = 1216;
constexpr int L_REC = L_XN, REC_STR = 1536;
constexpr int L_ACT = L_XN + 8 * HN_STR, ACT_STR = 144;
constexpr int L_RED = L_ACT + 8 * ACT_STR;
constexpr int L_Q = L_RED + 1152;
constexpr int L_FLG = L_Q + 512;
constexpr int L_END = L_FLG + 256;
static_assert(L_END <= 163840, "LDS");
constexpr int L_SCR = L_END;
constexpr int L_TOTAL = L_SCR + 2048 + 256;
static_assert(L_TOTAL <= 163840, "LDS");
constexpr int F_QREADY = 0, F_QDONE = 1, F_CHAIN = 2, F_TICK = 3, F_XN = 5, F_FULL = 6, F_TAG = 8, F_S0 = 10, F_FIN = 12, F_END = 14;
#define CODE_PF 3
#define TLB_PF 1
#define EXP 0
#define MAPV 0
#define PACE 0
#define TAU 56
#define T_TARGET 3900
#define LEAD_F 0
#define LEAD_O 0
constexpr u64 TAG_STEP = 0x9E3779ull << 40 | 0x16Bull << 30 | 0x2D5ull << 20 | 0x0F3ull << 10 | 0x1B5ull;
#define TAG_FIELDS(t) v4u{(unsigned)(t) & 0x3ffu, ((unsigned)(t) >> 10) & 0x3ffu, ((unsigned)(t) >> 20) & 0x3ffu, (unsigned)((t) >> 30) & 0x3ffu}
#define WQ_AHEAD 12
#define ROT_ON 1
#define WINDOW 9
#define WIN() asm volatile("s_waitcnt vmcnt(" STR(WINDOW) ")" ::: "memory")
#define KAUX 2
#define VAUX 2
#define WAUX 2
#define KPOL "nt"
#define VPOL "nt"
#define WPOL "nt"
#define PPOLL_AUX AUX_SC1
#define QPOLL_AUX AUX_SC1
#define QPOLL_DELAY 50
#define MAXL 8
#define DMA_INFL 12
#define STR2(x) #x
#define STR(x) STR2(x)

constexpr int AUX_NT = 2, AUX_SC1 = 16, AUX_SC1NT = 18;
constexpr unsigned OOB = 0x7FFFFFF0u;
constexpr int SPIN_CAP = 1 << 17, LDS_SPIN_CAP = 1 << 22;
constexpr float LOG2E = 1.4426950408889634f;

#define SB() __builtin_amdgcn_sched_barrier(0)
#define CBAR() asm volatile("" ::: "memory")
#define WG_BARRIER() do { asm volatile("s_waitcnt lgkmcnt(0)\n\ts_barrier" ::: "memory"); } while (0)

__device__ __forceinline__ f32x4 mfma32(v4u a, v4u b, f32x4 c) {
  return __builtin_amdgcn_mfma_f32_16x16x32_bf16(__builtin_bit_cast(bf16x8, a), __builtin_bit_cast(bf16x8, b), c, 0, 0, 0);
}
__device__ __forceinline__ f32x4 mfma16(v2u a, v2u b, f32x4 c) {
  return __builtin_amdgcn_mfma_f32_16x16x16bf16_1k(__builtin_bit_cast(s16x4, a), __builtin_bit_cast(s16x4, b), c, 0, 0, 0);
}
__device__ __forceinline__ unsigned cvtpk(float lo, float hi) {
  f32x2_t v = {lo, hi};
  return __builtin_bit_cast(unsigned, __builtin_convertvector(v, bf16x2_t));
}
__device__ __forceinline__ float rbf(float x) { return __uint_as_float(cvtpk(x, x) & 0xffff0000u); }
__device__ __forceinline__ float bflo(unsigned x) { return __uint_as_float(x << 16); }
__device__ __forceinline__ float bfhi(unsigned x) { return __uint_as_float(x & 0xffff0000u); }
template <int CTRL> __device__ __forceinline__ float dppf(float x) {
  return __builtin_bit_cast(float, __builtin_amdgcn_mov_dpp(__builtin_bit_cast(int, x), CTRL, 0xf, 0xf, true));
}
template <int CTRL> __device__ __forceinline__ unsigned dppu(unsigned x) { return (unsigned)__builtin_amdgcn_mov_dpp((int)x, CTRL, 0xf, 0xf, true); }
constexpr int QP_XOR1 = 0xB1, QP_XOR2 = 0x4E, ROR4 = 0x124, ROR8 = 0x128;
constexpr int QP_0011 = 0x50, QP_2233 = 0xFA, QP_0000 = 0x00, QP_1111 = 0x55, QP_ID = 0xE4;
__device__ __forceinline__ float xrow_max(float x) {
  auto s = __builtin_amdgcn_permlane16_swap(__float_as_uint(x), __float_as_uint(x), false, false);
  x = fmaxf(__uint_as_float(s[0]), __uint_as_float(s[1]));
  auto t = __builtin_amdgcn_permlane32_swap(__float_as_uint(x), __float_as_uint(x), false, false);
  return fmaxf(__uint_as_float(t[0]), __uint_as_float(t[1]));
}
__device__ __forceinline__ float xrow_sum(float x) {
  auto s = __builtin_amdgcn_permlane16_swap(__float_as_uint(x), __float_as_uint(x), false, false);
  x = __uint_as_float(s[0]) + __uint_as_float(s[1]);
  auto t = __builtin_amdgcn_permlane32_swap(__float_as_uint(x), __float_as_uint(x), false, false);
  return __uint_as_float(t[0]) + __uint_as_float(t[1]);
}
__device__ __forceinline__ float row2_sum(float x) {
  auto s = __builtin_amdgcn_permlane16_swap(__float_as_uint(x), __float_as_uint(x), false, false);
  return __uint_as_float(s[0]) + __uint_as_float(s[1]);
}
__device__ __forceinline__ float wave_sum(float x) {
  x += dppf<QP_XOR1>(x); x += dppf<QP_XOR2>(x); x += dppf<ROR4>(x); x += dppf<ROR8>(x);
  return xrow_sum(x);
}
template <int SRC, int DST> __device__ __forceinline__ unsigned bank_mov(unsigned old, unsigned x) {
  constexpr int sh = ((DST - SRC) & 3) * 4;
  constexpr int ctrl = sh == 0 ? QP_ID : (0x120 + sh);
  return (unsigned)__builtin_amdgcn_update_dpp((int)old, (int)x, ctrl, 0xf, 1 << DST, false);
}
__device__ __forceinline__ rsrc_t mkrs(const void* p, unsigned bytes) { return __builtin_amdgcn_make_buffer_rsrc((void*)p, 0, bytes, 0x00020000); }
__device__ __forceinline__ v2u trread(LAS unsigned char* p) {
  return __builtin_bit_cast(v2u, __builtin_amdgcn_ds_read_tr16_b64_v4i16((LAS s16x4*)p));
}
__device__ __forceinline__ v4u trfrag(LAS unsigned char* p, int rowstride4) {
  const v2u a = trread(p), b = trread(p + rowstride4);
  return v4u{a.x, a.y, b.x, b.y};
}
__device__ __forceinline__ v4u tagged(f32x4 v, v4u t) {
  return v4u{(__float_as_uint(v.x) & ~0x3ffu) | t.x, (__float_as_uint(v.y) & ~0x3ffu) | t.y, (__float_as_uint(v.z) & ~0x3ffu) | t.z, (__float_as_uint(v.w) & ~0x3ffu) | t.w};
}
__device__ __forceinline__ void untag(v4u& v, v4u t, unsigned& bad) { v ^= t; bad |= (v.x | v.y) | (v.z | v.w); }
__device__ __forceinline__ void lds_wait_ge(volatile LAS unsigned* p, unsigned v) {
  for (int s = 0; *p < v && s < LDS_SPIN_CAP; ++s) __builtin_amdgcn_s_sleep(1);
  CBAR();
}
__device__ __forceinline__ void lds_spin_ge(volatile LAS unsigned* p, unsigned v) {
  for (int s = 0; *p < v && s < LDS_SPIN_CAP; ++s) { }
  CBAR();
}
__device__ __forceinline__ void ln_row(const float (&v)[12], bool live, v2u (&out)[3]) {
  const float ref = __uint_as_float(__builtin_amdgcn_readfirstlane(__float_as_uint(v[0])));
  float s1 = 0.f, s2 = 0.f;
#pragma unroll
  for (int i = 0; i < 12; ++i) { const float d = live ? v[i] - ref : 0.f; s1 += d; s2 = fmaf(d, d, s2); }
  s1 = wave_sum(s1); s2 = wave_sum(s2);
  const float md = s1 * (1.f / 576.f);
  const float var = fmaxf(fmaf(-md, md, s2 * (1.f / 576.f)), 0.f);
  const float m = rbf(ref + md);
  const float den = rbf(__builtin_amdgcn_sqrtf(rbf(rbf(var) + 1e-5f)));
  const float inv = __builtin_amdgcn_rcpf(den);
#pragma unroll
  for (int i = 0; i < 3; ++i) {
    const unsigned a = cvtpk(v[4 * i] - m, v[4 * i + 1] - m), c = cvtpk(v[4 * i + 2] - m, v[4 * i + 3] - m);
    out[i] = v2u{cvtpk(bflo(a) * inv, bfhi(a) * inv), cvtpk(bflo(c) * inv, bfhi(c) * inv)};
  }
}

#define STAMP(i) do { } while (0)

__global__ void __launch_bounds__(512, 2) fused_block(const u16* __restrict__ x, const u16* __restrict__ Kc, const u16* __restrict__ Vc, const u16* __restrict__ wq,
    const u16* __restrict__ wo, const u16* __restrict__ wgt, const u16* __restrict__ wup, const u16* __restrict__ wdn, u16* out, unsigned char* ws) {
  __shared__ __attribute__((aligned(16))) unsigned char smem_[L_TOTAL];
  LAS unsigned char* L = (LAS unsigned char*)smem_;
  volatile LAS unsigned* FLG = (volatile LAS unsigned*)(L + L_FLG);
  const int tid = threadIdx.x, lane = tid & 63, w = __builtin_amdgcn_readfirstlane(tid >> 6);
#define MKDESC(p, bytes) v4u{(unsigned)(u64)(p), (unsigned)((u64)(p) >> 32) & 0xffffu, (unsigned)(bytes), 0x00020000u}
  const unsigned lbase = (unsigned)(size_t)L;
#define DMA_ASMP(pol, desc, ldsa, voff, soff, imm) asm volatile("s_mov_b32 m0, %0\n\ts_nop 0\n\tbuffer_load_dwordx4 %1, %2, %3 offen offset:%4 " pol " lds" \
    :: "s"((ldsa) - (imm)), "v"(voff), "s"(desc), "s"(soff), "i"(imm) : "memory")
#define DMA_ASM(desc, ldsa, voff, soff, imm) DMA_ASMP(WPOL, desc, ldsa, voff, soff, imm)
  const bool live = lane < 48, two = (w == 2 || w == 7);
  const unsigned sw2 = (unsigned)((((lane & 7) ^ (((lane >> 4) & 1) << 1)) * 16) ^ (w << 6));
  v2u ep, xpk[3], xp2[3];
  if (w < 2) {
    {
      u64 pc; unsigned len;
      asm volatile("s_getpc_b64 %0\n.Lfb_code_begin:\n\ts_mov_b32 %1, .Lfb_code_end-.Lfb_code_begin" : "=s"(pc), "=s"(len));
      const v4u dc = MKDESC(pc, len);
      asm volatile("s_mov_b32 m0, %0\n\ts_nop 0\n\tbuffer_load_dword %1, %2, %3 offen lds\n\tbuffer_load_dword %1, %2, %4 offen lds"
        :: "s"(lbase + L_SCR + 2048), "v"(lane * 128), "s"(dc), "s"(w * 8192), "s"(w * 8192 + 16384) : "memory"); }
    const v4u dq = MKDESC(wq, 576 * 4096 * 2);
    const unsigned vo = (unsigned)((blockIdx.x & 3) * 144 + w * 8 + (lane >> 3)) * 8192u + (blockIdx.x >> 2) * 128 + sw2;
#pragma unroll
    for (int i = 0; i < 9; ++i) DMA_ASM(dq, lbase + L_WQ + w * 1024 + i * 2048, vo, i * 16 * 8192, 0);
    asm volatile("" : "=v"(ep), "=v"(xpk[0]), "=v"(xpk[1]), "=v"(xpk[2]), "=v"(xp2[0]), "=v"(xp2[1]), "=v"(xp2[2]));
  } else {
    const unsigned xo = (unsigned)(8 * (live ? lane : 47)), xo1 = xo + w * 1152, xo2 = xo + (w & 1) * 1152, z = 0;
    asm volatile("global_load_dwordx2 %0, %3, %4\n\tglobal_load_dwordx2 %1, %3, %4 offset:384\n\tglobal_load_dwordx2 %2, %3, %4 offset:768"
      : "=&v"(xpk[0]), "=&v"(xpk[1]), "=&v"(xpk[2]) : "v"(xo1), "s"(x) : "memory");
    asm volatile("global_load_dwordx2 %0, %1, %2 sc1" : "=&v"(ep) : "v"(z), "s"(ws) : "memory");
    if (two) asm volatile("global_load_dwordx2 %0, %3, %4\n\tglobal_load_dwordx2 %1, %3, %4 offset:384\n\tglobal_load_dwordx2 %2, %3, %4 offset:768"
      : "=&v"(xp2[0]), "=&v"(xp2[1]), "=&v"(xp2[2]) : "v"(xo2), "s"(x) : "memory");
    else asm volatile("" : "=v"(xp2[0]), "=v"(xp2[1]), "=v"(xp2[2]));
    if (w == 2) FLG[lane] = 0u;
  }
  WG_BARRIER();
  int wg = blockIdx.x;
  asm volatile("" : "+s"(wg));
  const int uu = MAPV == 0 ? wg : (MAPV == 1 ? (wg & 7) * 32 + (wg >> 3) : (MAPV == 2 ? ((wg * 37 + (wg >> 3) * 0) & 255) ^ ((wg >> 3) & 7) : (MAPV == 3 ? (wg ^ ((wg >> 3) & 7) ^ ((wg >> 6) & 3)) : (int)(__builtin_bitreverse32(wg) >> 24))));
  const int pair = uu >> 1, half = uu & 1, b = pair >> 4, kvh = pair & 15;
  const int rp = (pair & 7) * 16 + (pair >> 3);
  const int hsel = (pair >> 3) & 1;
  const bool holder = half == hsel;
  const bool isF = holder && rp < 72, isO = holder && rp >= 72 && rp < 108;
  const int fu = rp;
  const int sk = (rp - 72) & 3, sn = (rp - 72) >> 2;
  const int cb = wg >> 2, kq = wg & 3;
  const int n = lane & 15, g = lane >> 4, tq_ = (lane & 15) >> 2, tp = lane & 3;
  const int f2 = (tq_ >> 1) | ((g & 1) << 1);
  const rsrc_t rws = mkrs(ws, WS_END);
  const int dl = rp < 72 ? DELTA_F : (rp < 108 ? DELTA_O : 0);
  const int nt0 = 64 + (hsel ? dl : -dl);
  const int NT = half ? 128 - nt0 : nt0;
  const unsigned tbase = ((unsigned)pair << 20) + (half ? (unsigned)nt0 * 8192u : 0u);
  const int voffK = (n >> 1) * 256 + (2 * g + (n & 1)) * 16;
  const int voffV = g * 2048 + n * 16;
  v4u kr[8], vr[8];
#define KOFF(i) (voffK + (((i) >> 1) & 1) * 2048 + ((i) & 1) * 128)
#define LDK(i, so) kr[i] = __builtin_amdgcn_raw_buffer_load_b128(rk, KOFF(i), (so) + ((i) >> 2) * 4096, KAUX)
#define LDV(i, so) vr[i] = __builtin_amdgcn_raw_buffer_load_b128(rv, voffV + (i) * 256, (so), VAUX)
  const rsrc_t rk = mkrs(Kc, 128u << 20), rv = mkrs(Vc, 128u << 20);
#define DMA_PACE() asm volatile("s_waitcnt vmcnt(" STR(DMA_INFL) ")" ::: "memory")
#define LDS_INC(f) do { if (lane == 0) __hip_atomic_fetch_add((LAS unsigned*)(FLG + (f)), 1u, __ATOMIC_RELAXED, __HIP_MEMORY_SCOPE_WORKGROUP); } while (0)
  const rsrc_t rx = mkrs(x, 8 * 576 * 2);
  u64 tag = 0;
  const unsigned guvo = (unsigned)(lane >> 3) * 9216u + fu * 128 + (((lane & 7) ^ ((lane >> 4) & 1)) * 16);
#define GU_DMA(i) do { const int mt_ = (i) >= 72, ri_ = (i) - 72 * mt_; const v4u dg_ = MKDESC(mt_ ? wup : wgt, 576 * 4608 * 2); \
    DMA_ASM(dg_, lbase + (i) * 1024, guvo ^ (unsigned)(((ri_ & 1) << 5) | (mt_ << 6)), ri_ * 8 * 9216, 0); } while (0)
  const int tl0 = isF ? N_EARLY * 1024 : (isO ? 131072 : 0);
  const int dbk = w - 2;
#define DOWN_LOADS() do { const rsrc_t rd = mkrs(wdn, dsz); \
    const unsigned vo_ = (dbk < 4 || n < 8) ? (unsigned)(fu * 64 + 8 * g) * 1152u + dbk * 256 + n * 16 : OOB; \
    _Pragma("unroll") for (int i = 0; i < 16; ++i) df[i] = __builtin_amdgcn_raw_buffer_load_b128(rd, vo_, ((i >> 3) * 32 + (i & 7)) * 1152, WAUX); } while (0)
#define DOWN_TRANSPOSE() do { _Pragma("unroll") for (int ks = 0; ks < 2; ++ks) { v4u t_[8]; \
      _Pragma("unroll") for (int jj = 0; jj < 8; ++jj) _Pragma("unroll") for (int mm = 0; mm < 4; ++mm) \
        t_[jj][mm] = __builtin_amdgcn_perm(df[ks * 8 + 2 * mm + 1][jj >> 1], df[ks * 8 + 2 * mm][jj >> 1], (jj & 1) ? 0x07060302u : 0x05040100u); \
      _Pragma("unroll") for (int jj = 0; jj < 8; ++jj) { df[ks * 8 + jj] = t_[jj]; asm volatile("" : "+v"(df[ks * 8 + jj])); } } } while (0)

  const int nrounds = max((147456 - 4096 - tl0) >> 15, 0);
  if (w < 2) {
    static_assert(DMA_INFL == 12, "the wq signal sits behind the 12th piece");
    const int nw = isF ? N_EARLY : (isO ? 128 : 0);
#pragma unroll 1
    for (int run = 0; run < 2; ++run) {
      const int p0 = isF ? 72 * run : 64 * run, p1 = min(nw, isF ? p0 + 72 : p0 + 64);
      if (p1 <= p0) break;
      const v4u dw = isF ? MKDESC(run ? wup : wgt, 576 * 4608 * 2) : MKDESC(wo, 4096 * 576 * 2);
      const unsigned vo = isF ? (guvo ^ (unsigned)((w << 5) | (run << 6))) : (unsigned)(sk * 1024 + (lane >> 3)) * 1152u + sn * 128 + sw2;
      const unsigned sstep = isF ? 16u * 9216u : 16u * 1152u;
      unsigned so = (isF ? 0u : (unsigned)p0 * 8u * 1152u) + (sstep >> 1) * w, la = lbase + (p0 + w) * 1024;
      if (run) lds_wait_ge(FLG + F_QDONE, 4u);
#pragma unroll 1
      for (int i = p0; i < p1; i += 8) {
#pragma unroll
        for (int j = 0; j < 4; ++j) { DMA_ASM(dw, la, vo, so, 0); DMA_PACE(); la += 2048; so += sstep; }
        if (i == 16) LDS_INC(F_XN);
      }
    }
    const v4u dk = MKDESC(Kc, 128u << 20), dv = MKDESC(Vc, 128u << 20);
#pragma unroll 1
    for (int rd = 0; rd < nrounds; ++rd) {
      const int slot = 2 * rd + w;
      if (tl0 + 4096 + (slot + 1) * 16384 > L_WQ) lds_wait_ge(FLG + F_QDONE, 4u);
      const unsigned so = tbase + (unsigned)slot * 8192u, la = lbase + tl0 + 4096 + slot * 16384;
#pragma unroll
      for (int i = 0; i < 8; ++i) { DMA_ASMP(KPOL, dk, la + i * 1024, voffK, so + (i >> 2) * 4096, ((i >> 1) & 1) * 2048 + (i & 1) * 128); DMA_PACE(); }
#pragma unroll
      for (int i = 0; i < 8; ++i) { DMA_ASMP(VPOL, dv, la + 8192 + i * 1024, voffV, so, i * 256); DMA_PACE();
        if (i == 3 && rd == 0 && nw == 0) LDS_INC(F_XN); }
      FLG[F_FULL + w] = rd;
    }
    STAMP(3);
    SB();
#pragma unroll
    for (int i = 0; i < 8; ++i) { LDK(i, tbase + (2 * nrounds + w) * 8192); SB(); DMA_PACE(); }
#pragma unroll
    for (int i = 0; i < 8; ++i) { LDV(i, tbase + (2 * nrounds + w) * 8192); SB(); DMA_PACE(); }
    FLG[F_FULL + w] = nrounds;
  } else {
    if (two) asm volatile("s_waitcnt vmcnt(4)" ::: "memory"); else asm volatile("s_waitcnt vmcnt(1)" ::: "memory");
    asm volatile("" : "+v"(xpk[0]), "+v"(xpk[1]), "+v"(xpk[2]));
#define LN_TO_XN(src, row) do { float v[12]; \
      _Pragma("unroll") for (int i = 0; i < 3; ++i) { v[4 * i] = bflo(src[i].x); v[4 * i + 1] = bfhi(src[i].x); v[4 * i + 2] = bflo(src[i].y); v[4 * i + 3] = bfhi(src[i].y); } \
      v2u o3[3]; ln_row(v, live, o3); \
      if (live) { _Pragma("unroll") for (int i = 0; i < 3; ++i) *(LAS v2u*)(L + L_XN + (row) * XN_STR + (192 * i + 4 * lane) * 2) = o3[i]; } } while (0)
    LN_TO_XN(xpk, w);
    SB();
    asm volatile("s_waitcnt vmcnt(0)" : "+v"(ep), "+v"(xp2[0]), "+v"(xp2[1]), "+v"(xp2[2]) :: "memory");
    if (two) LN_TO_XN(xp2, w & 1);
    asm volatile("s_waitcnt lgkmcnt(0)" ::: "memory");
    LDS_INC(F_XN);
    tag = ((u64)(unsigned)__builtin_amdgcn_readfirstlane(ep.y) << 32 | (unsigned)__builtin_amdgcn_readfirstlane(ep.x)) + TAG_STEP;
    const v4u tg = TAG_FIELDS(tag);
    if (w == 7) { FLG[F_TAG] = (unsigned)tag; FLG[F_TAG + 1] = (unsigned)(tag >> 32); }
    const v4u tq4 = tg & 15u;
    if (!two) {
      lds_spin_ge(FLG + F_XN, 8u);
      const int tq = w - 3;
      v4u a[5], bb[5];
#pragma unroll
      for (int ks = 0; ks < 5; ++ks) {
        const int gg = ks < 4 ? g : (g & 1);
        a[ks] = trfrag(L + L_WQ + (32 * ks + 8 * gg + tq_) * 128 + ((tq ^ f2) * 32) + tp * 8, 4 * 128);
        bb[ks] = *(LAS v4u*)(L + L_XN + (n & 7) * XN_STR + (kq * 144 + 32 * ks + 8 * gg) * 2);
      }
      SB();
      if (g >= 2) bb[4] = v4u{0, 0, 0, 0};
      f32x4 d = {0.f, 0.f, 0.f, 0.f};
#pragma unroll
      for (int ks = 0; ks < 5; ++ks) d = mfma32(a[ks], bb[ks], d);
      __builtin_amdgcn_raw_buffer_store_b128((__builtin_bit_cast(v4u, d) & ~15u) | tq4, rws, n < 8 ? WS_QG + ((n * 4 + kq) * 4096 + cb * 64 + tq * 16 + 4 * g) * 4 : OOB, 0, AUX_SC1);
      LDS_INC(F_QDONE);
      STAMP(2);
    }
    if (w == 7) {
      const int qo = WS_QG + (b * 4 * 4096 + kvh * 256 + 4 * lane) * 4;
      v4u a[4];
      if (QPOLL_DELAY) __builtin_amdgcn_s_sleep(QPOLL_DELAY);
#pragma unroll 1
      for (int s = 0; s < SPIN_CAP; ++s) {
#pragma unroll
        for (int i = 0; i < 4; ++i) a[i] = __builtin_amdgcn_raw_buffer_load_b128(rws, qo, i * 16384, QPOLL_AUX);
        unsigned bad = 0;
#pragma unroll
        for (int i = 0; i < 4; ++i) untag(a[i], tq4, bad);
        bad |= dppu<QP_XOR1>(bad); bad |= dppu<QP_XOR2>(bad);
        if ((bad & 15u) == 0u) break;
        __builtin_amdgcn_s_sleep(2);
        CBAR();
      }
#define QV(p) __uint_as_float((p) | 8u)
#define QS(c) ((QV(a[0].c) + QV(a[1].c)) + (QV(a[2].c) + QV(a[3].c)))
      *(LAS v2u*)(L + L_Q + lane * 8) = v2u{cvtpk(QS(x), QS(y)), cvtpk(QS(z), QS(w))};
      asm volatile("s_waitcnt lgkmcnt(0)" ::: "memory");
      FLG[F_QREADY] = 1u;
      STAMP(2);
      {
        unsigned j0 = __builtin_amdgcn_raw_buffer_load_b32(rws, lane < 3 ? WS_O + lane * (768 << 10) : OOB, 0, AUX_NT);
        unsigned j1 = __builtin_amdgcn_raw_buffer_load_b32(mkrs(out, 8 * 576 * 2), lane == 0 ? 0 : OOB, 0, AUX_NT);
        unsigned j2 = __builtin_amdgcn_raw_buffer_load_b32(mkrs(wdn, 4608 * 576 * 2), (isF && lane < 2) ? (fu * 64 + lane * 63) * 1152 : OOB, 0, AUX_NT);
        asm volatile("" :: "v"(j0), "v"(j1), "v"(j2)); }
    }
#pragma unroll
    for (int i = 0; i < 8; ++i) asm volatile("" : "=v"(kr[i]), "=v"(vr[i]));
  }
  if (w != 7) {
    lds_wait_ge(FLG + F_QREADY, 1u);
    STAMP(4);
    v4u qf[8];
    { const v4u qa0 = *(LAS v4u*)(L + L_Q + (((n >> 1) & 1) * 128 + 16 * g + 8 * (n & 1)) * 2), qa1 = *(LAS v4u*)(L + L_Q + (((n >> 1) & 1) * 128 + 64 + 16 * g + 8 * (n & 1)) * 2);
#pragma unroll
      for (int i = 0; i < 8; ++i) qf[i] = ((n >> 2) == (i >> 1)) ? ((i & 1) ? qa1 : qa0) : v4u{0, 0, 0, 0}; }
    const bool par = lane & 1;
    const unsigned jbmask = par ? 0xffff0000u : 0x0000ffffu;
    float m = -INFINITY, l = 0.f;
    f32x4 o = {0.f, 0.f, 0.f, 0.f};
    v4u pa0 = {0, 0, 0, 0}, pa1 = {0, 0, 0, 0};
#define PV(i, pa, xs_, MORE) { pa.x = bank_mov<((i) >> 1), 0>(pa.x, xs_); pa.y = bank_mov<((i) >> 1), 1>(pa.y, xs_); pa.z = bank_mov<((i) >> 1), 2>(pa.z, xs_); pa.w = bank_mov<((i) >> 1), 3>(pa.w, xs_); \
        o = mfma32(pa, vr[i], o); if (MORE) { WIN(); LDV(i, so); } SB(); }
#define BODY(MORE) { \
      f32x4 d = {0.f, 0.f, 0.f, 0.f}; \
      _Pragma("unroll") for (int i = 0; i < 8; ++i) { d = mfma32(qf[i], kr[i], d); if (MORE) { WIN(); LDK(i, so); } SB(); } \
      const float keep = par ? d.w : d.x, send = par ? d.y : d.z; \
      float s = keep + dppf<QP_XOR1>(send); \
      s = rbf(rbf(s) * 0.08838834764831845f); \
      if (__any(s > m)) { \
        float t = s; t = fmaxf(t, dppf<QP_XOR2>(t)); t = fmaxf(t, dppf<ROR4>(t)); t = fmaxf(t, dppf<ROR8>(t)); t = xrow_max(t); \
        const float mn = fmaxf(m, t); \
        const float al = __builtin_amdgcn_exp2f((m - mn) * LOG2E); \
        m = mn; l *= al; \
        const float a0 = dppf<QP_0000>(al), a1 = dppf<QP_1111>(al); \
        o.x *= a0; o.y *= a0; o.z *= a1; o.w *= a1; \
      } \
      const float p = __builtin_amdgcn_exp2f((s - m) * LOG2E); \
      l += p; \
      const unsigned p2 = cvtpk(p, p); \
      const unsigned x0 = dppu<QP_0011>(p2) & jbmask, x1 = dppu<QP_2233>(p2) & jbmask; \
      PV(0, pa0, x0, MORE) PV(1, pa1, x1, MORE) PV(2, pa0, x0, MORE) PV(3, pa1, x1, MORE) PV(4, pa0, x0, MORE) PV(5, pa1, x1, MORE) PV(6, pa0, x0, MORE) PV(7, pa1, x1, MORE) }
    if (w < 2) {
#pragma unroll 1
      for (int tnext = 2 * nrounds + 2 + w; tnext < NT; tnext += 2) { const unsigned so = tbase + (unsigned)tnext * 8192u;
        BODY(true)
      }
    }
#define REC_O(r) (*(LAS f32x4*)(L + L_REC + (r) * REC_STR + lane * 16))
#define REC_ML(r) (*(LAS f32x2_t*)(L + L_REC + (r) * REC_STR + 1024 + lane * 8))
#define MERGE(ro, rml) do { const float mn_ = fmaxf(m, rml.x), ms_ = mn_ == -INFINITY ? 0.f : mn_; \
      const float ea_ = __builtin_amdgcn_exp2f((m - ms_) * LOG2E), eb_ = __builtin_amdgcn_exp2f((rml.x - ms_) * LOG2E); \
      m = mn_; l = __fadd_rn(__fmul_rn(l, ea_), __fmul_rn(rml.y, eb_)); \
      const float a0_ = dppf<QP_0000>(ea_), a1_ = dppf<QP_1111>(ea_), b0_ = dppf<QP_0000>(eb_), b1_ = dppf<QP_1111>(eb_); \
      o.x = __fadd_rn(__fmul_rn(o.x, a0_), __fmul_rn(ro.x, b0_)); o.y = __fadd_rn(__fmul_rn(o.y, a0_), __fmul_rn(ro.y, b0_)); \
      o.z = __fadd_rn(__fmul_rn(o.z, a1_), __fmul_rn(ro.z, b1_)); o.w = __fadd_rn(__fmul_rn(o.w, a1_), __fmul_rn(ro.w, b1_)); } while (0)
    if (w == 0 && nrounds > 0) {
      lds_wait_ge(FLG + F_CHAIN, 5u);
      const f32x4 ro = REC_O(2); const f32x2_t rml = REC_ML(2); MERGE(ro, rml);
    }
    const u64 tagp = (u64)(unsigned)__builtin_amdgcn_readfirstlane(FLG[F_TAG + 1]) << 32 | (unsigned)__builtin_amdgcn_readfirstlane(FLG[F_TAG]);
#pragma unroll 1
    for (int t = w < 2 ? -1 : w - 2; t < 2 * nrounds; t += 5) {
      if (t >= 0) {
        lds_wait_ge(FLG + F_FULL + (t & 1), (unsigned)((t >> 1) + 1));
#pragma unroll
        for (int i = 0; i < 8; ++i) { kr[i] = *(LAS v4u*)(L + tl0 + 4096 + t * 16384 + i * 1024 + lane * 16); vr[i] = *(LAS v4u*)(L + tl0 + 4096 + t * 16384 + 8192 + i * 1024 + lane * 16); }
      }
      const unsigned so = 0; BODY(false)
      if (t < 0) break;
    }
    STAMP(5);
    bool pub = true;
    if (w >= 2) {
      if (w == 2) lds_wait_ge(FLG + F_QDONE, 4u);
      else lds_wait_ge(FLG + F_CHAIN, (unsigned)(w - 2));
    } else {
      if (FLG[F_TICK] == 0u) {
        REC_O(w) = o; REC_ML(w) = f32x2_t{m, l};
        asm volatile("s_waitcnt lgkmcnt(0)" ::: "memory");
        unsigned tk = 0;
        if (lane == 0) tk = __hip_atomic_fetch_add((LAS unsigned*)(FLG + F_TICK), 1u, __ATOMIC_RELAXED, __HIP_MEMORY_SCOPE_WORKGROUP);
        pub = __builtin_amdgcn_readfirstlane(tk) != 0u;
      }
      CBAR();
    }
    if (pub) {
      if (w != 2) { const int r = w < 2 ? 1 - w : 2; const f32x4 ro = REC_O(r); const f32x2_t rml = REC_ML(r); MERGE(ro, rml); }
      if (w >= 2) { REC_O(2) = o; REC_ML(2) = f32x2_t{m, l}; asm volatile("s_waitcnt lgkmcnt(0)" ::: "memory"); FLG[F_CHAIN] = w - 1; }
      const rsrc_t rp_ = mkrs(ws, w < 2 ? WS_END : 0);
      const v4u tg = TAG_FIELDS(tagp);
      l += dppf<QP_XOR2>(l); l += dppf<ROR4>(l); l += dppf<ROR8>(l); l = xrow_sum(l);
      const float inv = __builtin_amdgcn_rcpf(l), i0 = dppf<QP_0000>(inv), i1 = dppf<QP_1111>(inv);
      const int slot = kvh * 16 + b * 2 + half;
      __builtin_amdgcn_raw_buffer_store_b128(tagged(f32x4{o.x * i0, o.y * i0, o.z * i1, o.w * i1}, tg), rp_, WS_O + slot * 1024 + (n >> 2) * 256 + g * 64 + (n & 3) * 16, 0, AUX_SC1);
      const float lse = fmaf(m, LOG2E, __builtin_amdgcn_logf(l)), lse1 = dppf<QP_1111>(lse);
      __builtin_amdgcn_raw_buffer_store_b128(tagged(f32x4{lse, __uint_as_float(__float_as_uint(lse) << 22), lse1, __uint_as_float(__float_as_uint(lse1) << 22)}, tg), rp_, lane == 0 ? WS_LSE + slot * 16 : OOB, 0, AUX_SC1);
      __builtin_amdgcn_raw_buffer_store_b32((unsigned)(tagp >> 32), rp_, lane == 0 ? WS_DONE + wg * 4 : OOB, 0, AUX_SC1);
    }
  }
#pragma unroll 1
  for (int real = (w == 7) ? 0 : 1; real < 2; ++real) {
  const rsrc_t rt = mkrs(ws, real ? WS_END : 0);
  int lane_ = tid & 63;
  asm volatile("" : "+v"(lane_));
  const int lane = lane_, n = lane & 15, g = lane >> 4, tq_ = (lane & 15) >> 2, tp = lane & 3, f2 = (tq_ >> 1) | ((g & 1) << 1);
  const bool live = lane < 48;
  if (real) WG_BARRIER();
  STAMP(7);
  tag = real ? ((u64)(unsigned)__builtin_amdgcn_readfirstlane(FLG[F_TAG + 1]) << 32 | (unsigned)__builtin_amdgcn_readfirstlane(FLG[F_TAG])) : 0ull;
  const v4u tg = TAG_FIELDS(tag);
  const unsigned thint = (unsigned)(tag >> 32);
  if (isO) {
    v4u af[16];
#pragma unroll
    for (int i = 0; i < 16; ++i) af[i] = trfrag(L + L_WO + ((w >> 1) * 256 + (i >> 3) * 128 + (w & 1) * 64 + ((i >> 2) & 1) * 32 + 8 * g + tq_) * 128 + (((i & 3) ^ f2) * 32) + tp * 8, 4 * 128);
    const int so_ = (sk * 4 + (w >> 1)) * 16 + n;
    v4u of[8], ls;
#pragma unroll 1
    for (int s = 0; s < SPIN_CAP; ++s) {
      ls = __builtin_amdgcn_raw_buffer_load_b128(rt, WS_LSE + so_ * 16, 0, AUX_SC1);
#pragma unroll
      for (int i = 0; i < 8; ++i) of[i] = __builtin_amdgcn_raw_buffer_load_b128(rt, WS_O + so_ * 1024 + (w & 1) * 512 + g * 16 + i * 64, 0, AUX_SC1);
      unsigned bad = 0;
      untag(ls, tg, bad);
#pragma unroll
      for (int i = 0; i < 8; ++i) untag(of[i], tg, bad);
      if ((bad & 0x3ffu) == 0u) break;
      __builtin_amdgcn_s_sleep(1);
      CBAR();
    }
    STAMP(9);
#define U2F __uint_as_float
    f32x4 d[8];
#pragma unroll
    for (int t = 0; t < 8; ++t) d[t] = f32x4{0.f, 0.f, 0.f, 0.f};
#pragma unroll
    for (int ks = 0; ks < 2; ++ks) {
      const v4u b0 = {cvtpk(U2F(of[4 * ks].x), U2F(of[4 * ks].y)), cvtpk(U2F(of[4 * ks + 1].x), U2F(of[4 * ks + 1].y)), cvtpk(U2F(of[4 * ks + 2].x), U2F(of[4 * ks + 2].y)), cvtpk(U2F(of[4 * ks + 3].x), U2F(of[4 * ks + 3].y))};
      const v4u b1 = {cvtpk(U2F(of[4 * ks].z), U2F(of[4 * ks].w)), cvtpk(U2F(of[4 * ks + 1].z), U2F(of[4 * ks + 1].w)), cvtpk(U2F(of[4 * ks + 2].z), U2F(of[4 * ks + 2].w)), cvtpk(U2F(of[4 * ks + 3].z), U2F(of[4 * ks + 3].w))};
#pragma unroll
      for (int t = 0; t < 4; ++t) { d[t] = mfma32(af[ks * 4 + t], b0, d[t]); d[4 + t] = mfma32(af[8 + ks * 4 + t], b1, d[4 + t]); }
    }
    const float e0 = U2F(ls.x | (ls.y >> 22)), e1 = U2F(ls.z | (ls.w >> 22));
    const float w0 = __builtin_amdgcn_rcpf(1.f + __builtin_amdgcn_exp2f(dppf<QP_XOR1>(e0) - e0)), w1 = __builtin_amdgcn_rcpf(1.f + __builtin_amdgcn_exp2f(dppf<QP_XOR1>(e1) - e1));
    {
      const bool od = n & 1;
      f32x4 r[4];
#pragma unroll
      for (int t = 0; t < 4; ++t) r[t] = w0 * d[t] + w1 * d[4 + t];
      f32x4 k0 = od ? r[2] : r[0], k1 = od ? r[3] : r[1], s0 = od ? r[0] : r[2], s1 = od ? r[1] : r[3];
      k0.x += dppf<QP_XOR1>(s0.x); k0.y += dppf<QP_XOR1>(s0.y); k0.z += dppf<QP_XOR1>(s0.z); k0.w += dppf<QP_XOR1>(s0.w);
      k1.x += dppf<QP_XOR1>(s1.x); k1.y += dppf<QP_XOR1>(s1.y); k1.z += dppf<QP_XOR1>(s1.z); k1.w += dppf<QP_XOR1>(s1.w);
      LAS unsigned char* dst = L + (real ? L_RED1 + w * 2048 : L_SCR) + (n >> 1) * 256 + (od ? 128 : 0) + g * 16;
      *(LAS f32x4*)dst = k0; *(LAS f32x4*)(dst + 64) = k1; }
    if (real) WG_BARRIER();
    {
      f32x4 t = *(LAS f32x4*)(L + L_RED1 + g * 4096 + w * 256 + n * 16) + *(LAS f32x4*)(L + L_RED1 + g * 4096 + 2048 + w * 256 + n * 16);
      t.x = xrow_sum(t.x); t.y = xrow_sum(t.y); t.z = xrow_sum(t.z); t.w = xrow_sum(t.w);
      __builtin_amdgcn_raw_buffer_store_b128(tagged(t, tg), rt, lane < 16 ? WS_H + ((w * 4 + sk) * 576 + sn * 64 + n * 4) * 4 : OOB, 0, AUX_SC1);
      STAMP(10);
    }
  } else if (isF) {
    v4u df[16];
    const bool dwave = (w >= 2 && w < 7) || !real;
    if (N_EARLY < 144 && real) {
#pragma unroll 1
      for (int i = N_EARLY + w; i < 144; i += 8) GU_DMA(i);
    }
    if (dwave) { const unsigned dsz = real ? 4608 * 576 * 2 : 0; DOWN_LOADS(); DOWN_TRANSPOSE(); }
    else {
#pragma unroll
      for (int i = 0; i < 16; ++i) asm volatile("" : "=v"(df[i]));
    }
    if (N_EARLY < 144 && real) { asm volatile("s_waitcnt vmcnt(0)" ::: "memory"); WG_BARRIER(); }
    STAMP(6);
    SB();
    v2u xpk[3];
#pragma unroll
    for (int i = 0; i < 3; ++i) xpk[i] = __builtin_amdgcn_raw_buffer_load_b64(rx, 8 * (live ? lane : 47) + 384 * i, w * 1152, 0);
    v4u gf[18];
    { LAS unsigned char* ga = L + ((tp & 1) ? L_UP : L_GATE) + (8 * g + tq_) * 128 + ((w ^ (f2 | ((tp & 1) << 2))) * 16) + (tp >> 1) * 8;
#pragma unroll
      for (int ks = 0; ks < 18; ++ks) gf[ks] = trfrag(ga + ks * 32 * 128, 4 * 128); }
#pragma unroll
    for (int ks = 0; ks < 18; ++ks) asm volatile("" : "+v"(gf[ks]));
    asm volatile("" : "+v"(xpk[0]), "+v"(xpk[1]), "+v"(xpk[2]));
    if (w == 7) {
#pragma unroll 1
      for (int s = 0; s < SPIN_CAP; ++s) {
        const v4u f0 = __builtin_amdgcn_raw_buffer_load_b128(rt, WS_DONE + lane * 16, 0, AUX_SC1);
        if (((f0.x ^ thint) | (f0.y ^ thint) | (f0.z ^ thint) | (f0.w ^ thint)) == 0u) break;
        __builtin_amdgcn_s_sleep(8);
        CBAR();
      }
    }
    if (real) WG_BARRIER();
    int hoff[3];
#pragma unroll
    for (int i = 0; i < 3; ++i) { const int c = 24 * i + (lane >> 1); hoff[i] = (c & 3) * 288 + (c >> 2) * 16 + (lane & 1) * 8; asm volatile("" : "+v"(hoff[i])); }
    v4u hp[12];
#pragma unroll 1
    for (int s = 0; s < SPIN_CAP; ++s) {
#pragma unroll
      for (int i = 0; i < 12; ++i) hp[i] = __builtin_amdgcn_raw_buffer_load_b128(rt, WS_H + (w * 4 * 576 + 4 * (live ? lane : 47)) * 4, i * 768, AUX_SC1);
      unsigned bad = 0;
#pragma unroll
      for (int i = 0; i < 12; ++i) untag(hp[i], tg, bad);
      if ((bad & 0x3ffu) == 0u) break;
      CBAR();
    }
    STAMP(9);
    { float v[12];
#pragma unroll
      for (int i = 0; i < 3; ++i) {
        const f32x4 a = (__builtin_bit_cast(f32x4, hp[i]) + __builtin_bit_cast(f32x4, hp[3 + i])) + (__builtin_bit_cast(f32x4, hp[6 + i]) + __builtin_bit_cast(f32x4, hp[9 + i]));
        const unsigned r0 = cvtpk(a.x, a.y), r1 = cvtpk(a.z, a.w);
        const unsigned h0 = cvtpk(bflo(xpk[i].x) + bflo(r0), bfhi(xpk[i].x) + bfhi(r0)), h1 = cvtpk(bflo(xpk[i].y) + bflo(r1), bfhi(xpk[i].y) + bfhi(r1));
        v[4 * i] = bflo(h0); v[4 * i + 1] = bfhi(h0); v[4 * i + 2] = bflo(h1); v[4 * i + 3] = bfhi(h1); }
      v2u o3[3]; ln_row(v, live, o3);
      if (live) {
#pragma unroll
        for (int i = 0; i < 3; ++i) *(LAS v2u*)(L + (real ? L_XN + w * HN_STR : L_SCR) + hoff[i]) = o3[i]; } }
    if (real) WG_BARRIER();
    STAMP(11);
    {
      v4u hb[9];
#pragma unroll
      for (int ks = 0; ks < 9; ++ks) hb[ks] = *(LAS v4u*)(L + L_XN + (n & 7) * HN_STR + g * 288 + (n >> 3) * 144 + ks * 16);
      SB();
      f32x4 d = {0.f, 0.f, 0.f, 0.f}, d2 = {0.f, 0.f, 0.f, 0.f};
#pragma unroll
      for (int ks = 0; ks < 9; ++ks) { d = mfma32(gf[ks], hb[ks], d); d2 = mfma32(gf[9 + ks], hb[ks], d2); }
      d.x += dppf<ROR8>(d2.x); d.y += dppf<ROR8>(d2.y); d.z += dppf<ROR8>(d2.z); d.w += dppf<ROR8>(d2.w);
      auto s0 = __builtin_amdgcn_permlane16_swap(__float_as_uint(d.x), __float_as_uint(d.z), false, false);
      auto s1 = __builtin_amdgcn_permlane16_swap(__float_as_uint(d.y), __float_as_uint(d.w), false, false);
      float av[2];
#pragma unroll
      for (int e = 0; e < 2; ++e) {
        const float gg = rbf(__uint_as_float(e ? s1[0] : s0[0])), uu = rbf(__uint_as_float(e ? s1[1] : s0[1]));
        const float z2 = gg * fmaf(gg * gg, 0.044715f, 1.f) * (2.f * 0.7978845608028654f * LOG2E);
        const float ge = rbf(gg * __builtin_amdgcn_rcpf(1.f + __builtin_amdgcn_exp2f(-z2)));
        av[e] = ge * uu; }
      unsigned pk = cvtpk(av[0], av[1]);
      asm volatile("" : "+v"(pk));
      if (n < 8) *(LAS unsigned*)(L + L_ACT + n * ACT_STR + (8 * w + 2 * g) * 2) = pk; }
    if (real) WG_BARRIER();
    STAMP(12);
    if (dwave) {
      const v4u b0 = *(LAS v4u*)(L + L_ACT + (n & 7) * ACT_STR + g * 16), b1 = *(LAS v4u*)(L + L_ACT + (n & 7) * ACT_STR + 64 + g * 16);
      f32x4 d[8];
#pragma unroll
      for (int jj = 0; jj < 8; ++jj) d[jj] = mfma32(df[jj], b0, f32x4{0.f, 0.f, 0.f, 0.f});
#pragma unroll
      for (int jj = 0; jj < 8; ++jj) d[jj] = mfma32(df[8 + jj], b1, d[jj]);
      const bool hq = n >= 8;
      const unsigned po = (dbk < 4 || g < 2) ? WS_P + ((32 * dbk + 8 * g + (n >> 3)) * 72 + fu) * 128 + (n & 7) * 16 : OOB;
#pragma unroll
      for (int e = 0; e < 4; ++e)
        __builtin_amdgcn_raw_buffer_store_b128(tagged(f32x4{hq ? d[4][e] : d[0][e], hq ? d[5][e] : d[1][e], hq ? d[6][e] : d[2][e], hq ? d[7][e] : d[3][e]}, tg), rt, po, 2 * e * 72 * 128, AUX_SC1);
    }
  } else {
    const int unit = rp < 108 ? rp : 108 + (rp - 108) * 2 + half;
    if (unit < 144) {
      const int ob_ = lane & 7, oc = unit * 4;
      v2u xo = {0, 0};
      f32x4 a = {0.f, 0.f, 0.f, 0.f};
      if (w == 7) {
        xo = *(const GAS v2u*)(x + ob_ * 576 + oc);
        v4u hv;
#pragma unroll 1
        for (int s = 0; s < SPIN_CAP; ++s) {
          hv = __builtin_amdgcn_raw_buffer_load_b128(rt, WS_H + ((ob_ * 4 + ((lane >> 3) & 3)) * 576 + oc) * 4, 0, AUX_SC1);
          unsigned bad = 0; untag(hv, tg, bad);
          if ((bad & 0x3ffu) == 0u) break;
          __builtin_amdgcn_s_sleep(16);
          CBAR();
        }
        a = __builtin_bit_cast(f32x4, hv);
        a.x += dppf<ROR8>(a.x); a.y += dppf<ROR8>(a.y); a.z += dppf<ROR8>(a.z); a.w += dppf<ROR8>(a.w);
        a.x = row2_sum(a.x); a.y = row2_sum(a.y); a.z = row2_sum(a.z); a.w = row2_sum(a.w);
      }
      if (real) WG_BARRIER();
      STAMP(9);
      v4u p0, p1;
#pragma unroll 1
      for (int s = 0; s < SPIN_CAP; ++s) {
        p0 = __builtin_amdgcn_raw_buffer_load_b128(rt, WS_P + unit * 9216 + w * 1024 + lane * 16, 0, PPOLL_AUX);
        p1 = __builtin_amdgcn_raw_buffer_load_b128(rt, WS_P + unit * 9216 + 8192 + w * 128 + (lane & 7) * 16, 0, PPOLL_AUX);
        unsigned bad = 0; untag(p0, tg, bad); untag(p1, tg, bad);
        if ((bad & 0x3ffu) == 0u) break;
        __builtin_amdgcn_s_sleep(1);
        CBAR();
      }
      STAMP(11);
      f32x4 t = __builtin_bit_cast(f32x4, p0);
      t.x += dppf<ROR8>(t.x); t.y += dppf<ROR8>(t.y); t.z += dppf<ROR8>(t.z); t.w += dppf<ROR8>(t.w);
      t.x = xrow_sum(t.x); t.y = xrow_sum(t.y); t.z = xrow_sum(t.z); t.w = xrow_sum(t.w);
      if (lane < 8) *(LAS f32x4*)(L + L_RED + w * 128 + lane * 16) = t + __builtin_bit_cast(f32x4, p1);
      if (real) WG_BARRIER();
      if (w == 7 && lane < 8) {
        f32x4 f = *(LAS f32x4*)(L + L_RED + lane * 16);
#pragma unroll
        for (int r = 1; r < 8; ++r) f += *(LAS f32x4*)(L + L_RED + r * 128 + lane * 16);
        const unsigned r0 = cvtpk(a.x, a.y), r1 = cvtpk(a.z, a.w);
        const unsigned hh0 = cvtpk(bflo(xo.x) + bflo(r0), bfhi(xo.x) + bfhi(r0)), hh1 = cvtpk(bflo(xo.y) + bflo(r1), bfhi(xo.y) + bfhi(r1));
        const unsigned f0 = cvtpk(f.x, f.y), f1 = cvtpk(f.z, f.w);
        __builtin_amdgcn_raw_buffer_store_b64(v2u{cvtpk(bflo(hh0) + bflo(f0), bfhi(hh0) + bfhi(f0)), cvtpk(bflo(hh1) + bflo(f1), bfhi(hh1) + bfhi(f1))}, mkrs(out, real ? 8 * 576 * 2 : 0), (ob_ * 576 + oc) * 2, 0, 0);
        if (unit == 0 && lane == 0) __builtin_amdgcn_raw_buffer_store_b64(v2u{(unsigned)tag, (unsigned)(tag >> 32)}, rt, WS_EPOCH, 0, AUX_SC1);
        STAMP(15);
      }
    }
  }
  }
  asm volatile(".Lfb_code_end:");
}

extern "C" void kernel_launch(void* const* d_in, const int* in_sizes, int n_in, void* d_out, int out_size, void* d_ws, size_t ws_size, hipStream_t stream) {
  fused_block<<<256, 512, 0, stream>>>((const u16*)d_in[0], (const u16*)d_in[1], (const u16*)d_in[2], (const u16*)d_in[3], (const u16*)d_in[4],
      (const u16*)d_in[6], (const u16*)d_in[7], (const u16*)d_in[8], (u16*)d_out, (unsigned char*)d_ws);
}
```

```cpp
#include <hip/hip_runtime.h>
#include <hip/hip_bf16.h>

typedef unsigned short u16;
typedef unsigned long long u64;
typedef unsigned v4u __attribute__((ext_vector_type(4)));
typedef unsigned v2u __attribute__((ext_vector_type(2)));
typedef float f32x4 __attribute__((ext_vector_type(4)));
typedef short bf16x8 __attribute__((ext_vector_type(8)));
typedef short s16x4 __attribute__((ext_vector_type(4)));
typedef __bf16 bf16x2_t __attribute__((ext_vector_type(2)));
typedef float f32x2_t __attribute__((ext_vector_type(2)));
#define LAS __attribute__((address_space(3)))
#define GAS __attribute__((address_space(1)))
typedef __amdgpu_buffer_rsrc_t rsrc_t;

#define STAMPS 0
#define DELTA_F 4
#define DELTA_O 4
#define N_EARLY 144
#define DOWN_EARLY 0

constexpr int WS_EPOCH = 0;
constexpr int WS_DONE = 4096;
constexpr int WS_LSE = 8192;
constexpr int WS_H = 16384;
constexpr int WS_QG = 131072;
constexpr int WS_O = WS_QG + 8 * 4 * 4096 * 4;
constexpr int WS_P = WS_O + 32 * 16 * 512;
constexpr int WS_END = WS_P + 144 * 72 * 128;
constexpr int WS_DBG = 4 << 20;
constexpr int L_GATE = 0, L_UP = 73728, L_WO = 0, L_RED1 = 131072, L_WQ = 129024;
constexpr int L_XN = 147456, XN_STR = 1168, HN_STR = 1216;
constexpr int L_REC = L_XN, REC_STR = 1536;
constexpr int L_ACT = L_XN + 8 * HN_STR, ACT_STR = 144;
constexpr int L_RED = L_ACT + 8 * ACT_STR;
constexpr int L_Q = L_RED + 1152;
constexpr int L_FLG = L_Q + 512;
constexpr int L_END = L_FLG + 256;
static_assert(L_END <= 163840, "LDS");
constexpr int L_SCR = L_END;
constexpr int L_TOTAL = L_SCR + 2048 + 256;
static_assert(L_TOTAL <= 163840, "LDS");
constexpr int F_QREADY = 0, F_QDONE = 1, F_CHAIN = 2, F_TICK = 3, F_XN = 5, F_FULL = 6, F_TAG = 8, F_S0 = 10, F_FIN = 12, F_END = 14;
#define CODE_PF 3
#define TLB_PF 1
#define EXP 0
#define MAPV 0
#define PACE 0
#define TAU 56
#define T_TARGET 3900
#define LEAD_F 0
#define LEAD_O 0
constexpr u64 TAG_STEP = 0x9E3779ull << 40 | 0x16Bull << 30 | 0x2D5ull << 20 | 0x0F3ull << 10 | 0x1B5ull;
#define TAG_FIELDS(t) v4u{(unsigned)(t) & 0x3ffu, ((unsigned)(t) >> 10) & 0x3ffu, ((unsigned)(t) >> 20) & 0x3ffu, (unsigned)((t) >> 30) & 0x3ffu}
#define WQ_AHEAD 12
#define ROT_ON 1
#define WINDOW 9
#define WIN() asm volatile("s_waitcnt vmcnt(" STR(WINDOW) ")" ::: "memory")
#define KAUX 2
#define VAUX 2
#define WAUX 2
#define POL_0 ""
#define POL_2 "nt"
#define POL_3 "sc0 nt"
#define POL_18 "sc1 nt"
#define POL_19 "sc0 sc1 nt"
#define POL_1 "sc0"
#define POL_16 "sc1"
#define POL_17 "sc0 sc1"
#define POLCAT2(x) POL_##x
#define POLCAT(x) POLCAT2(x)
#define KPOL POLCAT(KAUX)
#define VPOL POLCAT(VAUX)
#define WPOL POLCAT(WAUX)
#define PPOLL_AUX AUX_SC1
#define QPOLL_AUX AUX_SC1
#define QPOLL_DELAY 50
#define MAXL 8
#define DMA_INFL 12
#define STR2(x) #x
#define STR(x) STR2(x)

constexpr int AUX_NT = 2, AUX_SC1 = 16, AUX_SC1NT = 18;
constexpr unsigned OOB = 0x7FFFFFF0u;
constexpr int SPIN_CAP = 1 << 17, LDS_SPIN_CAP = 1 << 22;
constexpr float LOG2E = 1.4426950408889634f;

#define SB() __builtin_amdgcn_sched_barrier(0)
#define CBAR() asm volatile("" ::: "memory")
#define WG_BARRIER() do { asm volatile("s_waitcnt lgkmcnt(0)\n\ts_barrier" ::: "memory"); } while (0)

__device__ __forceinline__ f32x4 mfma32(v4u a, v4u b, f32x4 c) {
  return __builtin_amdgcn_mfma_f32_16x16x32_bf16(__builtin_bit_cast(bf16x8, a), __builtin_bit_cast(bf16x8, b), c, 0, 0, 0);
}
__device__ __forceinline__ f32x4 mfma16(v2u a, v2u b, f32x4 c) {
  return __builtin_amdgcn_mfma_f32_16x16x16bf16_1k(__builtin_bit_cast(s16x4, a), __builtin_bit_cast(s16x4, b), c, 0, 0, 0);
}
__device__ __forceinline__ unsigned cvtpk(float lo, float hi) {
  f32x2_t v = {lo, hi};
  return __builtin_bit_cast(unsigned, __builtin_convertvector(v, bf16x2_t));
}
__device__ __forceinline__ float rbf(float x) { return __uint_as_float(cvtpk(x, x) & 0xffff0000u); }
__device__ __forceinline__ float bflo(unsigned x) { return __uint_as_float(x << 16); }
__device__ __forceinline__ float bfhi(unsigned x) { return __uint_as_float(x & 0xffff0000u); }
template <int CTRL> __device__ __forceinline__ float dppf(float x) {
  return __builtin_bit_cast(float, __builtin_amdgcn_mov_dpp(__builtin_bit_cast(int, x), CTRL, 0xf, 0xf, true));
}
template <int CTRL> __device__ __forceinline__ unsigned dppu(unsigned x) { return (unsigned)__builtin_amdgcn_mov_dpp((int)x, CTRL, 0xf, 0xf, true); }
constexpr int QP_XOR1 = 0xB1, QP_XOR2 = 0x4E, ROR4 = 0x124, ROR8 = 0x128;
constexpr int QP_0011 = 0x50, QP_2233 = 0xFA, QP_0000 = 0x00, QP_1111 = 0x55, QP_ID = 0xE4;
__device__ __forceinline__ float xrow_max(float x) {
  auto s = __builtin_amdgcn_permlane16_swap(__float_as_uint(x), __float_as_uint(x), false, false);
  x = fmaxf(__uint_as_float(s[0]), __uint_as_float(s[1]));
  auto t = __builtin_amdgcn_permlane32_swap(__float_as_uint(x), __float_as_uint(x), false, false);
  return fmaxf(__uint_as_float(t[0]), __uint_as_float(t[1]));
}
__device__ __forceinline__ float xrow_sum(float x) {
  auto s = __builtin_amdgcn_permlane16_swap(__float_as_uint(x), __float_as_uint(x), false, false);
  x = __uint_as_float(s[0]) + __uint_as_float(s[1]);
  auto t = __builtin_amdgcn_permlane32_swap(__float_as_uint(x), __float_as_uint(x), false, false);
  return __uint_as_float(t[0]) + __uint_as_float(t[1]);
}
__device__ __forceinline__ float row2_sum(float x) {
  auto s = __builtin_amdgcn_permlane16_swap(__float_as_uint(x), __float_as_uint(x), false, false);
  return __uint_as_float(s[0]) + __uint_as_float(s[1]);
}
__device__ __forceinline__ float wave_sum(float x) {
  x += dppf<QP_XOR1>(x); x += dppf<QP_XOR2>(x); x += dppf<ROR4>(x); x += dppf<ROR8>(x);
  return xrow_sum(x);
}
template <int SRC, int DST> __device__ __forceinline__ unsigned bank_mov(unsigned old, unsigned x) {
  constexpr int sh = ((DST - SRC) & 3) * 4;
  constexpr int ctrl = sh == 0 ? QP_ID : (0x120 + sh);
  return (unsigned)__builtin_amdgcn_update_dpp((int)old, (int)x, ctrl, 0xf, 1 << DST, false);
}
__device__ __forceinline__ rsrc_t mkrs(const void* p, unsigned bytes) { return __builtin_amdgcn_make_buffer_rsrc((void*)p, 0, bytes, 0x00020000); }
__device__ __forceinline__ v2u trread(LAS unsigned char* p) {
  return __builtin_bit_cast(v2u, __builtin_amdgcn_ds_read_tr16_b64_v4i16((LAS s16x4*)p));
}
__device__ __forceinline__ v4u trfrag(LAS unsigned char* p, int rowstride4) {
  const v2u a = trread(p), b = trread(p + rowstride4);
  return v4u{a.x, a.y, b.x, b.y};
}
__device__ __forceinline__ v4u tagged(f32x4 v, v4u t) {
  return v4u{(__float_as_uint(v.x) & ~0x3ffu) | t.x, (__float_as_uint(v.y) & ~0x3ffu) | t.y, (__float_as_uint(v.z) & ~0x3ffu) | t.z, (__float_as_uint(v.w) & ~0x3ffu) | t.w};
}
__device__ __forceinline__ void untag(v4u& v, v4u t, unsigned& bad) { v ^= t; bad |= (v.x | v.y) | (v.z | v.w); }
__device__ __forceinline__ void lds_wait_ge(volatile LAS unsigned* p, unsigned v) {
  for (int s = 0; *p < v && s < LDS_SPIN_CAP; ++s) __builtin_amdgcn_s_sleep(1);
  CBAR();
}
__device__ __forceinline__ void lds_spin_ge(volatile LAS unsigned* p, unsigned v) {
  for (int s = 0; *p < v && s < LDS_SPIN_CAP; ++s) { }
  CBAR();
}
__device__ __forceinline__ void ln_row(const float (&v)[12], bool live, v2u (&out)[3]) {
  const float ref = __uint_as_float(__builtin_amdgcn_readfirstlane(__float_as_uint(v[0])));
  float s1 = 0.f, s2 = 0.f;
#pragma unroll
  for (int i = 0; i < 12; ++i) { const float d = live ? v[i] - ref : 0.f; s1 += d; s2 = fmaf(d, d, s2); }
  s1 = wave_sum(s1); s2 = wave_sum(s2);
  const float md = s1 * (1.f / 576.f);
  const float var = fmaxf(fmaf(-md, md, s2 * (1.f / 576.f)), 0.f);
  const float m = rbf(ref + md);
  const float den = rbf(__builtin_amdgcn_sqrtf(rbf(rbf(var) + 1e-5f)));
  const float inv = __builtin_amdgcn_rcpf(den);
#pragma unroll
  for (int i = 0; i < 3; ++i) {
    const unsigned a = cvtpk(v[4 * i] - m, v[4 * i + 1] - m), c = cvtpk(v[4 * i + 2] - m, v[4 * i + 3] - m);
    out[i] = v2u{cvtpk(bflo(a) * inv, bfhi(a) * inv), cvtpk(bflo(c) * inv, bfhi(c) * inv)};
  }
}

#define STAMP(i) do { } while (0)

__global__ void __launch_bounds__(512, 2) fused_block(const u16* __restrict__ x, const u16* __restrict__ Kc, const u16* __restrict__ Vc, const u16* __restrict__ wq,
    const u16* __restrict__ wo, const u16* __restrict__ wgt, const u16* __restrict__ wup, const u16* __restrict__ wdn, u16* out, unsigned char* ws) {
  __shared__ __attribute__((aligned(16))) unsigned char smem_[L_TOTAL];
  LAS unsigned char* L = (LAS unsigned char*)smem_;
  volatile LAS unsigned* FLG = (volatile LAS unsigned*)(L + L_FLG);
  const int tid = threadIdx.x, lane = tid & 63, w = __builtin_amdgcn_readfirstlane(tid >> 6);
#define MKDESC(p, bytes) v4u{(unsigned)(u64)(p), (unsigned)((u64)(p) >> 32) & 0xffffu, (unsigned)(bytes), 0x00020000u}
  const unsigned lbase = (unsigned)(size_t)L;
#define DMA_ASMP(pol, desc, ldsa, voff, soff, imm) asm volatile("s_mov_b32 m0, %0\n\ts_nop 0\n\tbuffer_load_dwordx4 %1, %2, %3 offen offset:%4 " pol " lds" \
    :: "s"((ldsa) - (imm)), "v"(voff), "s"(desc), "s"(soff), "i"(imm) : "memory")
#define DMA_ASM(desc, ldsa, voff, soff, imm) DMA_ASMP(WPOL, desc, ldsa, voff, soff, imm)
  const bool live = lane < 48, two = (w == 2 || w == 7);
  const unsigned sw2 = (unsigned)((((lane & 7) ^ (((lane >> 4) & 1) << 1)) * 16) ^ (w << 6));
  v2u ep, xpk[3], xp2[3];
  if (w < 2) {
    {
      u64 pc; unsigned len;
      asm volatile("s_getpc_b64 %0\n.Lfb_code_begin:\n\ts_mov_b32 %1, .Lfb_code_end-.Lfb_code_begin" : "=s"(pc), "=s"(len));
      const v4u dc = MKDESC(pc, len);
      asm volatile("s_mov_b32 m0, %0\n\ts_nop 0\n\tbuffer_load_dword %1, %2, %3 offen lds\n\tbuffer_load_dword %1, %2, %4 offen lds"
        :: "s"(lbase + L_SCR + 2048), "v"(lane * 128), "s"(dc), "s"(w * 8192), "s"(w * 8192 + 16384) : "memory"); }
    const v4u dq = MKDESC(wq, 576 * 4096 * 2);
    const unsigned vo = (unsigned)((blockIdx.x & 3) * 144 + w * 8 + (lane >> 3)) * 8192u + (blockIdx.x >> 2) * 128 + sw2;
#pragma unroll
    for (int i = 0; i < 9; ++i) DMA_ASM(dq, lbase + L_WQ + w * 1024 + i * 2048, vo, i * 16 * 8192, 0);
    asm volatile("" : "=v"(ep), "=v"(xpk[0]), "=v"(xpk[1]), "=v"(xpk[2]), "=v"(xp2[0]), "=v"(xp2[1]), "=v"(xp2[2]));
  } else {
    const unsigned xo = (unsigned)(8 * (live ? lane : 47)), xo1 = xo + w * 1152, xo2 = xo + (w & 1) * 1152, z = 0;
    asm volatile("global_load_dwordx2 %0, %3, %4\n\tglobal_load_dwordx2 %1, %3, %4 offset:384\n\tglobal_load_dwordx2 %2, %3, %4 offset:768"
      : "=&v"(xpk[0]), "=&v"(xpk[1]), "=&v"(xpk[2]) : "v"(xo1), "s"(x) : "memory");
    asm volatile("global_load_dwordx2 %0, %1, %2 sc1" : "=&v"(ep) : "v"(z), "s"(ws) : "memory");
    if (two) asm volatile("global_load_dwordx2 %0, %3, %4\n\tglobal_load_dwordx2 %1, %3, %4 offset:384\n\tglobal_load_dwordx2 %2, %3, %4 offset:768"
      : "=&v"(xp2[0]), "=&v"(xp2[1]), "=&v"(xp2[2]) : "v"(xo2), "s"(x) : "memory");
    else asm volatile("" : "=v"(xp2[0]), "=v"(xp2[1]), "=v"(xp2[2]));
    if (w == 2) FLG[lane] = 0u;
  }
  WG_BARRIER();
  int wg = blockIdx.x;
  asm volatile("" : "+s"(wg));
  const int uu = MAPV == 0 ? wg : (MAPV == 1 ? (wg & 7) * 32 + (wg >> 3) : (MAPV == 2 ? ((wg * 37 + (wg >> 3) * 0) & 255) ^ ((wg >> 3) & 7) : (MAPV == 3 ? (wg ^ ((wg >> 3) & 7) ^ ((wg >> 6) & 3)) : (int)(__builtin_bitreverse32(wg) >> 24))));
  const int pair = uu >> 1, half = uu & 1, b = pair >> 4, kvh = pair & 15;
  const int rp = (pair & 7) * 16 + (pair >> 3);
  const int hsel = (pair >> 3) & 1;
  const bool holder = half == hsel;
  const bool isF = holder && rp < 72, isO = holder && rp >= 72 && rp < 108;
  const int fu = rp;
  const int sk = (rp - 72) & 3, sn = (rp - 72) >> 2;
  const int cb = wg >> 2, kq = wg & 3;
  const int n = lane & 15, g = lane >> 4, tq_ = (lane & 15) >> 2, tp = lane & 3;
  const int f2 = (tq_ >> 1) | ((g & 1) << 1);
  const rsrc_t rws = mkrs(ws, WS_END);
  const int dl = rp < 72 ? DELTA_F : (rp < 108 ? DELTA_O : 0);
  const int nt0 = 64 + (hsel ? dl : -dl);
  const int NT = half ? 128 - nt0 : nt0;
  const unsigned tbase = ((unsigned)pair << 20) + (half ? (unsigned)nt0 * 8192u : 0u);
  const int voffK = (n >> 1) * 256 + (2 * g + (n & 1)) * 16;
  const int voffV = g * 2048 + n * 16;
  v4u kr[8], vr[8];
#define KOFF(i) (voffK + (((i) >> 1) & 1) * 2048 + ((i) & 1) * 128)
#define LDK(i, so) kr[i] = __builtin_amdgcn_raw_buffer_load_b128(rk, KOFF(i), (so) + ((i) >> 2) * 4096, KAUX)
#define LDV(i, so) vr[i] = __builtin_amdgcn_raw_buffer_load_b128(rv, voffV + (i) * 256, (so), VAUX)
  const rsrc_t rk = mkrs(Kc, 128u << 20), rv = mkrs(Vc, 128u << 20);
#define DMA_PACE() asm volatile("s_waitcnt vmcnt(" STR(DMA_INFL) ")" ::: "memory")
#define LDS_INC(f) do { if (lane == 0) __hip_atomic_fetch_add((LAS unsigned*)(FLG + (f)), 1u, __ATOMIC_RELAXED, __HIP_MEMORY_SCOPE_WORKGROUP); } while (0)
  const rsrc_t rx = mkrs(x, 8 * 576 * 2);
  u64 tag = 0;
  const unsigned guvo = (unsigned)(lane >> 3) * 9216u + fu * 128 + (((lane & 7) ^ ((lane >> 4) & 1)) * 16);
#define GU_DMA(i) do { const int mt_ = (i) >= 72, ri_ = (i) - 72 * mt_; const v4u dg_ = MKDESC(mt_ ? wup : wgt, 576 * 4608 * 2); \
    DMA_ASM(dg_, lbase + (i) * 1024, guvo ^ (unsigned)(((ri_ & 1) << 5) | (mt_ << 6)), ri_ * 8 * 9216, 0); } while (0)
  const int tl0 = isF ? N_EARLY * 1024 : (isO ? 131072 : 0);
  const int dbk = w - 2;
#define DOWN_LOADS() do { const rsrc_t rd = mkrs(wdn, dsz); \
    const unsigned vo_ = (dbk < 4 || n < 8) ? (unsigned)(fu * 64 + 8 * g) * 1152u + dbk * 256 + n * 16 : OOB; \
    _Pragma("unroll") for (int i = 0; i < 16; ++i) df[i] = __builtin_amdgcn_raw_buffer_load_b128(rd, vo_, ((i >> 3) * 32 + (i & 7)) * 1152, WAUX); } while (0)
#define DOWN_TRANSPOSE() do { _Pragma("unroll") for (int ks = 0; ks < 2; ++ks) { v4u t_[8]; \
      _Pragma("unroll") for (int jj = 0; jj < 8; ++jj) _Pragma("unroll") for (int mm = 0; mm < 4; ++mm) \
        t_[jj][mm] = __builtin_amdgcn_perm(df[ks * 8 + 2 * mm + 1][jj >> 1], df[ks * 8 + 2 * mm][jj >> 1], (jj & 1) ? 0x07060302u : 0x05040100u); \
      _Pragma("unroll") for (int jj = 0; jj < 8; ++jj) { df[ks * 8 + jj] = t_[jj]; asm volatile("" : "+v"(df[ks * 8 + jj])); } } } while (0)

  const int nrounds = max((147456 - 4096 - tl0) >> 15, 0);
  if (w < 2) {
    static_assert(DMA_INFL == 12, "the wq signal sits behind the 12th piece");
    const int nw = isF ? N_EARLY : (isO ? 128 : 0);
#pragma unroll 1
    for (int run = 0; run < 2; ++run) {
      const int p0 = isF ? 72 * run : 64 * run, p1 = min(nw, isF ? p0 + 72 : p0 + 64);
      if (p1 <= p0) break;
      const v4u dw = isF ? MKDESC(run ? wup : wgt, 576 * 4608 * 2) : MKDESC(wo, 4096 * 576 * 2);
      const unsigned vo = isF ? (guvo ^ (unsigned)((w << 5) | (run << 6))) : (unsigned)(sk * 1024 + (lane >> 3)) * 1152u + sn * 128 + sw2;
      const unsigned sstep = isF ? 16u * 9216u : 16u * 1152u;
      unsigned so = (isF ? 0u : (unsigned)p0 * 8u * 1152u) + (sstep >> 1) * w, la = lbase + (p0 + w) * 1024;
      if (run) lds_wait_ge(FLG + F_QDONE, 4u);
#pragma unroll 1
      for (int i = p0; i < p1; i += 8) {
#pragma unroll
        for (int j = 0; j < 4; ++j) { DMA_ASM(dw, la, vo, so, 0); DMA_PACE(); la += 2048; so += sstep; }
        if (i == 16) LDS_INC(F_XN);
      }
    }
    const v4u dk = MKDESC(Kc, 128u << 20), dv = MKDESC(Vc, 128u << 20);
#pragma unroll 1
    for (int rd = 0; rd < nrounds; ++rd) {
      const int slot = 2 * rd + w;
      if (tl0 + 4096 + (slot + 1) * 16384 > L_WQ) lds_wait_ge(FLG + F_QDONE, 4u);
      const unsigned so = tbase + (unsigned)slot * 8192u, la = lbase + tl0 + 4096 + slot * 16384;
#pragma unroll
      for (int i = 0; i < 8; ++i) { DMA_ASMP(KPOL, dk, la + i * 1024, voffK, so + (i >> 2) * 4096, ((i >> 1) & 1) * 2048 + (i & 1) * 128); DMA_PACE(); }
#pragma unroll
      for (int i = 0; i < 8; ++i) { DMA_ASMP(VPOL, dv, la + 8192 + i * 1024, voffV, so, i * 256); DMA_PACE();
        if (i == 3 && rd == 0 && nw == 0) LDS_INC(F_XN); }
      FLG[F_FULL + w] = rd;
    }
    STAMP(3);
    SB();
#pragma unroll
    for (int i = 0; i < 8; ++i) { LDK(i, tbase + (2 * nrounds + w) * 8192); SB(); DMA_PACE(); }
#pragma unroll
    for (int i = 0; i < 8; ++i) { LDV(i, tbase + (2 * nrounds + w) * 8192); SB(); DMA_PACE(); }
    FLG[F_FULL + w] = nrounds;
  } else {
    if (two) asm volatile("s_waitcnt vmcnt(4)" ::: "memory"); else asm volatile("s_waitcnt vmcnt(1)" ::: "memory");
    asm volatile("" : "+v"(xpk[0]), "+v"(xpk[1]), "+v"(xpk[2]));
#define LN_TO_XN(src, row) do { float v[12]; \
      _Pragma("unroll") for (int i = 0; i < 3; ++i) { v[4 * i] = bflo(src[i].x); v[4 * i + 1] = bfhi(src[i].x); v[4 * i + 2] = bflo(src[i].y); v[4 * i + 3] = bfhi(src[i].y); } \
      v2u o3[3]; ln_row(v, live, o3); \
      if (live) { _Pragma("unroll") for (int i = 0; i < 3; ++i) *(LAS v2u*)(L + L_XN + (row) * XN_STR + (192 * i + 4 * lane) * 2) = o3[i]; } } while (0)
    LN_TO_XN(xpk, w);
    SB();
    asm volatile("s_waitcnt vmcnt(0)" : "+v"(ep), "+v"(xp2[0]), "+v"(xp2[1]), "+v"(xp2[2]) :: "memory");
    if (two) LN_TO_XN(xp2, w & 1);
    asm volatile("s_waitcnt lgkmcnt(0)" ::: "memory");
    LDS_INC(F_XN);
    tag = ((u64)(unsigned)__builtin_amdgcn_readfirstlane(ep.y) << 32 | (unsigned)__builtin_amdgcn_readfirstlane(ep.x)) + TAG_STEP;
    const v4u tg = TAG_FIELDS(tag);
    if (w == 7) { FLG[F_TAG] = (unsigned)tag; FLG[F_TAG + 1] = (unsigned)(tag >> 32); }
    const v4u tq4 = tg & 15u;
    if (!two) {
      lds_spin_ge(FLG + F_XN, 8u);
      const int tq = w - 3;
      v4u a[5], bb[5];
#pragma unroll
      for (int ks = 0; ks < 5; ++ks) {
        const int gg = ks < 4 ? g : (g & 1);
        a[ks] = trfrag(L + L_WQ + (32 * ks + 8 * gg + tq_) * 128 + ((tq ^ f2) * 32) + tp * 8, 4 * 128);
        bb[ks] = *(LAS v4u*)(L + L_XN + (n & 7) * XN_STR + (kq * 144 + 32 * ks + 8 * gg) * 2);
      }
      SB();
      if (g >= 2) bb[4] = v4u{0, 0, 0, 0};
      f32x4 d = {0.f, 0.f, 0.f, 0.f};
#pragma unroll
      for (int ks = 0; ks < 5; ++ks) d = mfma32(a[ks], bb[ks], d);
      __builtin_amdgcn_raw_buffer_store_b128((__builtin_bit_cast(v4u, d) & ~15u) | tq4, rws, n < 8 ? WS_QG + ((n * 4 + kq) * 4096 + cb * 64 + tq * 16 + 4 * g) * 4 : OOB, 0, AUX_SC1);
      LDS_INC(F_QDONE);
      STAMP(2);
    }
    if (w == 7) {
      const int qo = WS_QG + (b * 4 * 4096 + kvh * 256 + 4 * lane) * 4;
      v4u a[4];
      if (QPOLL_DELAY) __builtin_amdgcn_s_sleep(QPOLL_DELAY);
#pragma unroll 1
      for (int s = 0; s < SPIN_CAP; ++s) {
#pragma unroll
        for (int i = 0; i < 4; ++i) a[i] = __builtin_amdgcn_raw_buffer_load_b128(rws, qo, i * 16384, QPOLL_AUX);
        unsigned bad = 0;
#pragma unroll
        for (int i = 0; i < 4; ++i) untag(a[i], tq4, bad);
        bad |= dppu<QP_XOR1>(bad); bad |= dppu<QP_XOR2>(bad);
        if ((bad & 15u) == 0u) break;
        __builtin_amdgcn_s_sleep(2);
        CBAR();
      }
#define QV(p) __uint_as_float((p) | 8u)
#define QS(c) ((QV(a[0].c) + QV(a[1].c)) + (QV(a[2].c) + QV(a[3].c)))
      *(LAS v2u*)(L + L_Q + lane * 8) = v2u{cvtpk(QS(x), QS(y)), cvtpk(QS(z), QS(w))};
      asm volatile("s_waitcnt lgkmcnt(0)" ::: "memory");
      FLG[F_QREADY] = 1u;
      STAMP(2);
      {
        unsigned j0 = __builtin_amdgcn_raw_buffer_load_b32(rws, lane < 3 ? WS_O + lane * (768 << 10) : OOB, 0, AUX_NT);
        unsigned j1 = __builtin_amdgcn_raw_buffer_load_b32(mkrs(out, 8 * 576 * 2), lane == 0 ? 0 : OOB, 0, AUX_NT);
        unsigned j2 = __builtin_amdgcn_raw_buffer_load_b32(mkrs(wdn, 4608 * 576 * 2), (isF && lane < 2) ? (fu * 64 + lane * 63) * 1152 : OOB, 0, AUX_NT);
        asm volatile("" :: "v"(j0), "v"(j1), "v"(j2)); }
    }
#pragma unroll
    for (int i = 0; i < 8; ++i) asm volatile("" : "=v"(kr[i]), "=v"(vr[i]));
  }
  if (w != 7) {
    lds_wait_ge(FLG + F_QREADY, 1u);
    STAMP(4);
    v4u qf[8];
    { const v4u qa0 = *(LAS v4u*)(L + L_Q + (((n >> 1) & 1) * 128 + 16 * g + 8 * (n & 1)) * 2), qa1 = *(LAS v4u*)(L + L_Q + (((n >> 1) & 1) * 128 + 64 + 16 * g + 8 * (n & 1)) * 2);
#pragma unroll
      for (int i = 0; i < 8; ++i) qf[i] = ((n >> 2) == (i >> 1)) ? ((i & 1) ? qa1 : qa0) : v4u{0, 0, 0, 0}; }
    const bool par = lane & 1;
    const unsigned jbmask = par ? 0xffff0000u : 0x0000ffffu;
    float m = -INFINITY, l = 0.f;
    f32x4 o = {0.f, 0.f, 0.f, 0.f};
    v4u pa0 = {0, 0, 0, 0}, pa1 = {0, 0, 0, 0};
#define PV(i, pa, xs_, MORE) { pa.x = bank_mov<((i) >> 1), 0>(pa.x, xs_); pa.y = bank_mov<((i) >> 1), 1>(pa.y, xs_); pa.z = bank_mov<((i) >> 1), 2>(pa.z, xs_); pa.w = bank_mov<((i) >> 1), 3>(pa.w, xs_); \
        o = mfma32(pa, vr[i], o); if (MORE) { WIN(); LDV(i, so); } SB(); }
#define BODY(MORE) { \
      f32x4 d = {0.f, 0.f, 0.f, 0.f}; \
      _Pragma("unroll") for (int i = 0; i < 8; ++i) { d = mfma32(qf[i], kr[i], d); if (MORE) { WIN(); LDK(i, so); } SB(); } \
      const float keep = par ? d.w : d.x, send = par ? d.y : d.z; \
      float s = keep + dppf<QP_XOR1>(send); \
      s = rbf(rbf(s) * 0.08838834764831845f); \
      if (__any(s > m)) { \
        float t = s; t = fmaxf(t, dppf<QP_XOR2>(t)); t = fmaxf(t, dppf<ROR4>(t)); t = fmaxf(t, dppf<ROR8>(t)); t = xrow_max(t); \
        const float mn = fmaxf(m, t); \
        const float al = __builtin_amdgcn_exp2f((m - mn) * LOG2E); \
        m = mn; l *= al; \
        const float a0 = dppf<QP_0000>(al), a1 = dppf<QP_1111>(al); \
        o.x *= a0; o.y *= a0; o.z *= a1; o.w *= a1; \
      } \
      const float p = __builtin_amdgcn_exp2f((s - m) * LOG2E); \
      l += p; \
      const unsigned p2 = cvtpk(p, p); \
      const unsigned x0 = dppu<QP_0011>(p2) & jbmask, x1 = dppu<QP_2233>(p2) & jbmask; \
      PV(0, pa0, x0, MORE) PV(1, pa1, x1, MORE) PV(2, pa0, x0, MORE) PV(3, pa1, x1, MORE) PV(4, pa0, x0, MORE) PV(5, pa1, x1, MORE) PV(6, pa0, x0, MORE) PV(7, pa1, x1, MORE) }
    if (w < 2) {
#pragma unroll 1
      for (int tnext = 2 * nrounds + 2 + w; tnext < NT; tnext += 2) { const unsigned so = tbase + (unsigned)tnext * 8192u;
        BODY(true)
      }
    }
#define REC_O(r) (*(LAS f32x4*)(L + L_REC + (r) * REC_STR + lane * 16))
#define REC_ML(r) (*(LAS f32x2_t*)(L + L_REC + (r) * REC_STR + 1024 + lane * 8))
#define MERGE(ro, rml) do { const float mn_ = fmaxf(m, rml.x), ms_ = mn_ == -INFINITY ? 0.f : mn_; \
      const float ea_ = __builtin_amdgcn_exp2f((m - ms_) * LOG2E), eb_ = __builtin_amdgcn_exp2f((rml.x - ms_) * LOG2E); \
      m = mn_; l = __fadd_rn(__fmul_rn(l, ea_), __fmul_rn(rml.y, eb_)); \
      const float a0_ = dppf<QP_0000>(ea_), a1_ = dppf<QP_1111>(ea_), b0_ = dppf<QP_0000>(eb_), b1_ = dppf<QP_1111>(eb_); \
      o.x = __fadd_rn(__fmul_rn(o.x, a0_), __fmul_rn(ro.x, b0_)); o.y = __fadd_rn(__fmul_rn(o.y, a0_), __fmul_rn(ro.y, b0_)); \
      o.z = __fadd_rn(__fmul_rn(o.z, a1_), __fmul_rn(ro.z, b1_)); o.w = __fadd_rn(__fmul_rn(o.w, a1_), __fmul_rn(ro.w, b1_)); } while (0)
    if (w == 0 && nrounds > 0) {
      lds_wait_ge(FLG + F_CHAIN, 5u);
      const f32x4 ro = REC_O(2); const f32x2_t rml = REC_ML(2); MERGE(ro, rml);
    }
    const u64 tagp = (u64)(unsigned)__builtin_amdgcn_readfirstlane(FLG[F_TAG + 1]) << 32 | (unsigned)__builtin_amdgcn_readfirstlane(FLG[F_TAG]);
#pragma unroll 1
    for (int t = w < 2 ? -1 : w - 2; t < 2 * nrounds; t += 5) {
      if (t >= 0) {
        lds_wait_ge(FLG + F_FULL + (t & 1), (unsigned)((t >> 1) + 1));
#pragma unroll
        for (int i = 0; i < 8; ++i) { kr[i] = *(LAS v4u*)(L + tl0 + 4096 + t * 16384 + i * 1024 + lane * 16); vr[i] = *(LAS v4u*)(L + tl0 + 4096 + t * 16384 + 8192 + i * 1024 + lane * 16); }
      }
      const unsigned so = 0; BODY(false)
      if (t < 0) break;
    }
    STAMP(5);
    bool pub = true;
    if (w >= 2) {
      if (w == 2) lds_wait_ge(FLG + F_QDONE, 4u);
      else lds_wait_ge(FLG + F_CHAIN, (unsigned)(w - 2));
    } else {
      if (FLG[F_TICK] == 0u) {
        REC_O(w) = o; REC_ML(w) = f32x2_t{m, l};
        asm volatile("s_waitcnt lgkmcnt(0)" ::: "memory");
        unsigned tk = 0;
        if (lane == 0) tk = __hip_atomic_fetch_add((LAS unsigned*)(FLG + F_TICK), 1u, __ATOMIC_RELAXED, __HIP_MEMORY_SCOPE_WORKGROUP);
        pub = __builtin_amdgcn_readfirstlane(tk) != 0u;
      }
      CBAR();
    }
    if (pub) {
      if (w != 2) { const int r = w < 2 ? 1 - w : 2; const f32x4 ro = REC_O(r); const f32x2_t rml = REC_ML(r); MERGE(ro, rml); }
      if (w >= 2) { REC_O(2) = o; REC_ML(2) = f32x2_t{m, l}; asm volatile("s_waitcnt lgkmcnt(0)" ::: "memory"); FLG[F_CHAIN] = w - 1; }
      const rsrc_t rp_ = mkrs(ws, w < 2 ? WS_END : 0);
      const v4u tg = TAG_FIELDS(tagp);
      l += dppf<QP_XOR2>(l); l += dppf<ROR4>(l); l += dppf<ROR8>(l); l = xrow_sum(l);
      const float inv = __builtin_amdgcn_rcpf(l), i0 = dppf<QP_0000>(inv), i1 = dppf<QP_1111>(inv);
      const int slot = kvh * 16 + b * 2 + half;
      __builtin_amdgcn_raw_buffer_store_b128(tagged(f32x4{o.x * i0, o.y * i0, o.z * i1, o.w * i1}, tg), rp_, WS_O + slot * 1024 + (n >> 2) * 256 + g * 64 + (n & 3) * 16, 0, AUX_SC1);
      const float lse = fmaf(m, LOG2E, __builtin_amdgcn_logf(l)), lse1 = dppf<QP_1111>(lse);
      __builtin_amdgcn_raw_buffer_store_b128(tagged(f32x4{lse, __uint_as_float(__float_as_uint(lse) << 22), lse1, __uint_as_float(__float_as_uint(lse1) << 22)}, tg), rp_, lane == 0 ? WS_LSE + slot * 16 : OOB, 0, AUX_SC1);
      __builtin_amdgcn_raw_buffer_store_b32((unsigned)(tagp >> 32), rp_, lane == 0 ? WS_DONE + wg * 4 : OOB, 0, AUX_SC1);
    }
  }
#pragma unroll 1
  for (int real = (w == 7) ? 0 : 1; real < 2; ++real) {
  const rsrc_t rt = mkrs(ws, real ? WS_END : 0);
  int lane_ = tid & 63;
  asm volatile("" : "+v"(lane_));
  const int lane = lane_, n = lane & 15, g = lane >> 4, tq_ = (lane & 15) >> 2, tp = lane & 3, f2 = (tq_ >> 1) | ((g & 1) << 1);
  const bool live = lane < 48;
  if (real) WG_BARRIER();
  STAMP(7);
  tag = real ? ((u64)(unsigned)__builtin_amdgcn_readfirstlane(FLG[F_TAG + 1]) << 32 | (unsigned)__builtin_amdgcn_readfirstlane(FLG[F_TAG])) : 0ull;
  const v4u tg = TAG_FIELDS(tag);
  const unsigned thint = (unsigned)(tag >> 32);
  if (isO) {
    v4u af[16];
#pragma unroll
    for (int i = 0; i < 16; ++i) af[i] = trfrag(L + L_WO + ((w >> 1) * 256 + (i >> 3) * 128 + (w & 1) * 64 + ((i >> 2) & 1) * 32 + 8 * g + tq_) * 128 + (((i & 3) ^ f2) * 32) + tp * 8, 4 * 128);
    const int so_ = (sk * 4 + (w >> 1)) * 16 + n;
    v4u of[8], ls;
#pragma unroll 1
    for (int s = 0; s < SPIN_CAP; ++s) {
      ls = __builtin_amdgcn_raw_buffer_load_b128(rt, WS_LSE + so_ * 16, 0, AUX_SC1);
#pragma unroll
      for (int i = 0; i < 8; ++i) of[i] = __builtin_amdgcn_raw_buffer_load_b128(rt, WS_O + so_ * 1024 + (w & 1) * 512 + g * 16 + i * 64, 0, AUX_SC1);
      unsigned bad = 0;
      untag(ls, tg, bad);
#pragma unroll
      for (int i = 0; i < 8; ++i) untag(of[i], tg, bad);
      if ((bad & 0x3ffu) == 0u) break;
      __builtin_amdgcn_s_sleep(1);
      CBAR();
    }
    STAMP(9);
#define U2F __uint_as_float
    f32x4 d[8];
#pragma unroll
    for (int t = 0; t < 8; ++t) d[t] = f32x4{0.f, 0.f, 0.f, 0.f};
#pragma unroll
    for (int ks = 0; ks < 2; ++ks) {
      const v4u b0 = {cvtpk(U2F(of[4 * ks].x), U2F(of[4 * ks].y)), cvtpk(U2F(of[4 * ks + 1].x), U2F(of[4 * ks + 1].y)), cvtpk(U2F(of[4 * ks + 2].x), U2F(of[4 * ks + 2].y)), cvtpk(U2F(of[4 * ks + 3].x), U2F(of[4 * ks + 3].y))};
      const v4u b1 = {cvtpk(U2F(of[4 * ks].z), U2F(of[4 * ks].w)), cvtpk(U2F(of[4 * ks + 1].z), U2F(of[4 * ks + 1].w)), cvtpk(U2F(of[4 * ks + 2].z), U2F(of[4 * ks + 2].w)), cvtpk(U2F(of[4 * ks + 3].z), U2F(of[4 * ks + 3].w))};
#pragma unroll
      for (int t = 0; t < 4; ++t) { d[t] = mfma32(af[ks * 4 + t], b0, d[t]); d[4 + t] = mfma32(af[8 + ks * 4 + t], b1, d[4 + t]); }
    }
    const float e0 = U2F(ls.x | (ls.y >> 22)), e1 = U2F(ls.z | (ls.w >> 22));
    const float w0 = __builtin_amdgcn_rcpf(1.f + __builtin_amdgcn_exp2f(dppf<QP_XOR1>(e0) - e0)), w1 = __builtin_amdgcn_rcpf(1.f + __builtin_amdgcn_exp2f(dppf<QP_XOR1>(e1) - e1));
    {
      const bool od = n & 1;
      f32x4 r[4];
#pragma unroll
      for (int t = 0; t < 4; ++t) r[t] = w0 * d[t] + w1 * d[4 + t];
      f32x4 k0 = od ? r[2] : r[0], k1 = od ? r[3] : r[1], s0 = od ? r[0] : r[2], s1 = od ? r[1] : r[3];
      k0.x += dppf<QP_XOR1>(s0.x); k0.y += dppf<QP_XOR1>(s0.y); k0.z += dppf<QP_XOR1>(s0.z); k0.w += dppf<QP_XOR1>(s0.w);
      k1.x += dppf<QP_XOR1>(s1.x); k1.y += dppf<QP_XOR1>(s1.y); k1.z += dppf<QP_XOR1>(s1.z); k1.w += dppf<QP_XOR1>(s1.w);
      LAS unsigned char* dst = L + (real ? L_RED1 + w * 2048 : L_SCR) + (n >> 1) * 256 + (od ? 128 : 0) + g * 16;
      *(LAS f32x4*)dst = k0; *(LAS f32x4*)(dst + 64) = k1; }
    if (real) WG_BARRIER();
    {
      f32x4 t = *(LAS f32x4*)(L + L_RED1 + g * 4096 + w * 256 + n * 16) + *(LAS f32x4*)(L + L_RED1 + g * 4096 + 2048 + w * 256 + n * 16);
      t.x = xrow_sum(t.x); t.y = xrow_sum(t.y); t.z = xrow_sum(t.z); t.w = xrow_sum(t.w);
      __builtin_amdgcn_raw_buffer_store_b128(tagged(t, tg), rt, lane < 16 ? WS_H + ((w * 4 + sk) * 576 + sn * 64 + n * 4) * 4 : OOB, 0, AUX_SC1);
      STAMP(10);
    }
  } else if (isF) {
    v4u df[16];
    const bool dwave = (w >= 2 && w < 7) || !real;
    if (N_EARLY < 144 && real) {
#pragma unroll 1
      for (int i = N_EARLY + w; i < 144; i += 8) GU_DMA(i);
    }
    if (dwave) { const unsigned dsz = real ? 4608 * 576 * 2 : 0; DOWN_LOADS(); DOWN_TRANSPOSE(); }
    else {
#pragma unroll
      for (int i = 0; i < 16; ++i) asm volatile("" : "=v"(df[i]));
    }
    if (N_EARLY < 144 && real) { asm volatile("s_waitcnt vmcnt(0)" ::: "memory"); WG_BARRIER(); }
    STAMP(6);
    SB();
    v2u xpk[3];
#pragma unroll
    for (int i = 0; i < 3; ++i) xpk[i] = __builtin_amdgcn_raw_buffer_load_b64(rx, 8 * (live ? lane : 47) + 384 * i, w * 1152, 0);
    v4u gf[18];
    { LAS unsigned char* ga = L + ((tp & 1) ? L_UP : L_GATE) + (8 * g + tq_) * 128 + ((w ^ (f2 | ((tp & 1) << 2))) * 16) + (tp >> 1) * 8;
#pragma unroll
      for (int ks = 0; ks < 18; ++ks) gf[ks] = trfrag(ga + ks * 32 * 128, 4 * 128); }
#pragma unroll
    for (int ks = 0; ks < 18; ++ks) asm volatile("" : "+v"(gf[ks]));
    asm volatile("" : "+v"(xpk[0]), "+v"(xpk[1]), "+v"(xpk[2]));
    if (w == 7) {
#pragma unroll 1
      for (int s = 0; s < SPIN_CAP; ++s) {
        const v4u f0 = __builtin_amdgcn_raw_buffer_load_b128(rt, WS_DONE + lane * 16, 0, AUX_SC1);
        if (((f0.x ^ thint) | (f0.y ^ thint) | (f0.z ^ thint) | (f0.w ^ thint)) == 0u) break;
        __builtin_amdgcn_s_sleep(8);
        CBAR();
      }
    }
    if (real) WG_BARRIER();
    int hoff[3];
#pragma unroll
    for (int i = 0; i < 3; ++i) { const int c = 24 * i + (lane >> 1); hoff[i] = (c & 3) * 288 + (c >> 2) * 16 + (lane & 1) * 8; asm volatile("" : "+v"(hoff[i])); }
    v4u hp[12];
#pragma unroll 1
    for (int s = 0; s < SPIN_CAP; ++s) {
#pragma unroll
      for (int i = 0; i < 12; ++i) hp[i] = __builtin_amdgcn_raw_buffer_load_b128(rt, WS_H + (w * 4 * 576 + 4 * (live ? lane : 47)) * 4, i * 768, AUX_SC1);
      unsigned bad = 0;
#pragma unroll
      for (int i = 0; i < 12; ++i) untag(hp[i], tg, bad);
      if ((bad & 0x3ffu) == 0u) break;
      CBAR();
    }
    STAMP(9);
    { float v[12];
#pragma unroll
      for (int i = 0; i < 3; ++i) {
        const f32x4 a = (__builtin_bit_cast(f32x4, hp[i]) + __builtin_bit_cast(f32x4, hp[3 + i])) + (__builtin_bit_cast(f32x4, hp[6 + i]) + __builtin_bit_cast(f32x4, hp[9 + i]));
        const unsigned r0 = cvtpk(a.x, a.y), r1 = cvtpk(a.z, a.w);
        const unsigned h0 = cvtpk(bflo(xpk[i].x) + bflo(r0), bfhi(xpk[i].x) + bfhi(r0)), h1 = cvtpk(bflo(xpk[i].y) + bflo(r1), bfhi(xpk[i].y) + bfhi(r1));
        v[4 * i] = bflo(h0); v[4 * i + 1] = bfhi(h0); v[4 * i + 2] = bflo(h1); v[4 * i + 3] = bfhi(h1); }
      v2u o3[3]; ln_row(v, live, o3);
      if (live) {
#pragma unroll
        for (int i = 0; i < 3; ++i) *(LAS v2u*)(L + (real ? L_XN + w * HN_STR : L_SCR) + hoff[i]) = o3[i]; } }
    if (real) WG_BARRIER();
    STAMP(11);
    {
      v4u hb[9];
#pragma unroll
      for (int ks = 0; ks < 9; ++ks) hb[ks] = *(LAS v4u*)(L + L_XN + (n & 7) * HN_STR + g * 288 + (n >> 3) * 144 + ks * 16);
      SB();
      f32x4 d = {0.f, 0.f, 0.f, 0.f}, d2 = {0.f, 0.f, 0.f, 0.f};
#pragma unroll
      for (int ks = 0; ks < 9; ++ks) { d = mfma32(gf[ks], hb[ks], d); d2 = mfma32(gf[9 + ks], hb[ks], d2); }
      d.x += dppf<ROR8>(d2.x); d.y += dppf<ROR8>(d2.y); d.z += dppf<ROR8>(d2.z); d.w += dppf<ROR8>(d2.w);
      auto s0 = __builtin_amdgcn_permlane16_swap(__float_as_uint(d.x), __float_as_uint(d.z), false, false);
      auto s1 = __builtin_amdgcn_permlane16_swap(__float_as_uint(d.y), __float_as_uint(d.w), false, false);
      float av[2];
#pragma unroll
      for (int e = 0; e < 2; ++e) {
        const float gg = rbf(__uint_as_float(e ? s1[0] : s0[0])), uu = rbf(__uint_as_float(e ? s1[1] : s0[1]));
        const float z2 = gg * fmaf(gg * gg, 0.044715f, 1.f) * (2.f * 0.7978845608028654f * LOG2E);
        const float ge = rbf(gg * __builtin_amdgcn_rcpf(1.f + __builtin_amdgcn_exp2f(-z2)));
        av[e] = ge * uu; }
      unsigned pk = cvtpk(av[0], av[1]);
      asm volatile("" : "+v"(pk));
      if (n < 8) *(LAS unsigned*)(L + L_ACT + n * ACT_STR + (8 * w + 2 * g) * 2) = pk; }
    if (real) WG_BARRIER();
    STAMP(12);
    if (dwave) {
      const v4u b0 = *(LAS v4u*)(L + L_ACT + (n & 7) * ACT_STR + g * 16), b1 = *(LAS v4u*)(L + L_ACT + (n & 7) * ACT_STR + 64 + g * 16);
      f32x4 d[8];
#pragma unroll
      for (int jj = 0; jj < 8; ++jj) d[jj] = mfma32(df[jj], b0, f32x4{0.f, 0.f, 0.f, 0.f});
#pragma unroll
      for (int jj = 0; jj < 8; ++jj) d[jj] = mfma32(df[8 + jj], b1, d[jj]);
      const bool hq = n >= 8;
      const unsigned po = (dbk < 4 || g < 2) ? WS_P + ((32 * dbk + 8 * g + (n >> 3)) * 72 + fu) * 128 + (n & 7) * 16 : OOB;
#pragma unroll
      for (int e = 0; e < 4; ++e)
        __builtin_amdgcn_raw_buffer_store_b128(tagged(f32x4{hq ? d[4][e] : d[0][e], hq ? d[5][e] : d[1][e], hq ? d[6][e] : d[2][e], hq ? d[7][e] : d[3][e]}, tg), rt, po, 2 * e * 72 * 128, AUX_SC1);
    }
  } else {
    const int unit = rp < 108 ? rp : 108 + (rp - 108) * 2 + half;
    if (unit < 144) {
      const int ob_ = lane & 7, oc = unit * 4;
      v2u xo = {0, 0};
      f32x4 a = {0.f, 0.f, 0.f, 0.f};
      if (w == 7) {
        xo = *(const GAS v2u*)(x + ob_ * 576 + oc);
        v4u hv;
#pragma unroll 1
        for (int s = 0; s < SPIN_CAP; ++s) {
          hv = __builtin_amdgcn_raw_buffer_load_b128(rt, WS_H + ((ob_ * 4 + ((lane >> 3) & 3)) * 576 + oc) * 4, 0, AUX_SC1);
          unsigned bad = 0; untag(hv, tg, bad);
          if ((bad & 0x3ffu) == 0u) break;
          __builtin_amdgcn_s_sleep(16);
          CBAR();
        }
        a = __builtin_bit_cast(f32x4, hv);
        a.x += dppf<ROR8>(a.x); a.y += dppf<ROR8>(a.y); a.z += dppf<ROR8>(a.z); a.w += dppf<ROR8>(a.w);
        a.x = row2_sum(a.x); a.y = row2_sum(a.y); a.z = row2_sum(a.z); a.w = row2_sum(a.w);
      }
      if (real) WG_BARRIER();
      STAMP(9);
      v4u p0, p1;
#pragma unroll 1
      for (int s = 0; s < SPIN_CAP; ++s) {
        p0 = __builtin_amdgcn_raw_buffer_load_b128(rt, WS_P + unit * 9216 + w * 1024 + lane * 16, 0, PPOLL_AUX);
        p1 = __builtin_amdgcn_raw_buffer_load_b128(rt, WS_P + unit * 9216 + 8192 + w * 128 + (lane & 7) * 16, 0, PPOLL_AUX);
        unsigned bad = 0; untag(p0, tg, bad); untag(p1, tg, bad);
        if ((bad & 0x3ffu) == 0u) break;
        __builtin_amdgcn_s_sleep(1);
        CBAR();
      }
      STAMP(11);
      f32x4 t = __builtin_bit_cast(f32x4, p0);
      t.x += dppf<ROR8>(t.x); t.y += dppf<ROR8>(t.y); t.z += dppf<ROR8>(t.z); t.w += dppf<ROR8>(t.w);
      t.x = xrow_sum(t.x); t.y = xrow_sum(t.y); t.z = xrow_sum(t.z); t.w = xrow_sum(t.w);
      if (lane < 8) *(LAS f32x4*)(L + L_RED + w * 128 + lane * 16) = t + __builtin_bit_cast(f32x4, p1);
      if (real) WG_BARRIER();
      if (w == 7 && lane < 8) {
        f32x4 f = *(LAS f32x4*)(L + L_RED + lane * 16);
#pragma unroll
        for (int r = 1; r < 8; ++r) f += *(LAS f32x4*)(L + L_RED + r * 128 + lane * 16);
        const unsigned r0 = cvtpk(a.x, a.y), r1 = cvtpk(a.z, a.w);
        const unsigned hh0 = cvtpk(bflo(xo.x) + bflo(r0), bfhi(xo.x) + bfhi(r0)), hh1 = cvtpk(bflo(xo.y) + bflo(r1), bfhi(xo.y) + bfhi(r1));
        const unsigned f0 = cvtpk(f.x, f.y), f1 = cvtpk(f.z, f.w);
        __builtin_amdgcn_raw_buffer_store_b64(v2u{cvtpk(bflo(hh0) + bflo(f0), bfhi(hh0) + bfhi(f0)), cvtpk(bflo(hh1) + bflo(f1), bfhi(hh1) + bfhi(f1))}, mkrs(out, real ? 8 * 576 * 2 : 0), (ob_ * 576 + oc) * 2, 0, 0);
        if (unit == 0 && lane == 0) __builtin_amdgcn_raw_buffer_store_b64(v2u{(unsigned)tag, (unsigned)(tag >> 32)}, rt, WS_EPOCH, 0, AUX_SC1);
        STAMP(15);
      }
    }
  }
  }
  asm volatile(".Lfb_code_end:");
}

extern "C" void kernel_launch(void* const* d_in, const int* in_sizes, int n_in, void* d_out, int out_size, void* d_ws, size_t ws_size, hipStream_t stream) {
  fused_block<<<256, 512, 0, stream>>>((const u16*)d_in[0], (const u16*)d_in[1], (const u16*)d_in[2], (const u16*)d_in[3], (const u16*)d_in[4],
      (const u16*)d_in[6], (const u16*)d_in[7], (const u16*)d_in[8], (u16*)d_out, (unsigned char*)d_ws);
}
```

```cpp
#include <hip/hip_runtime.h>
#include <hip/hip_bf16.h>

typedef unsigned short u16;
typedef unsigned long long u64;
typedef unsigned v4u __attribute__((ext_vector_type(4)));
typedef unsigned v2u __attribute__((ext_vector_type(2)));
typedef float f32x4 __attribute__((ext_vector_type(4)));
typedef short bf16x8 __attribute__((ext_vector_type(8)));
typedef short s16x4 __attribute__((ext_vector_type(4)));
typedef __bf16 bf16x2_t __attribute__((ext_vector_type(2)));
typedef float f32x2_t __attribute__((ext_vector_type(2)));
#define LAS __attribute__((address_space(3)))
#define GAS __attribute__((address_space(1)))
typedef __amdgpu_buffer_rsrc_t rsrc_t;

#define STAMPS 0
#define DELTA_F 5
#define DELTA_O 4
#define N_EARLY 144
#define DOWN_EARLY 0

#define OPRIV 0
#define OSEG 1024
#define OROW 64
#define LSE_STR 16
#define WS_LSE_OFF (3 << 20)
constexpr int WS_EPOCH = 0;
constexpr int WS_DONE = 98304;
constexpr int WS_LSE = WS_LSE_OFF;
constexpr int WS_H = 16384;
constexpr int WS_QG = 131072;
constexpr int WS_O = WS_QG + 8 * 4 * 4096 * 4;
constexpr int WS_P = WS_O + 16 * 8192;
constexpr int WS_END = WS_LSE_OFF + 256 * 128;
constexpr int WS_DBG = 4 << 20;
constexpr int L_GATE = 0, L_UP = 73728, L_WO = 0, L_RED1 = 131072, L_WQ = 129024;
constexpr int L_XN = 147456, XN_STR = 1168, HN_STR = 1216;
constexpr int L_REC = L_XN, REC_STR = 1536;
constexpr int L_ACT = L_XN + 8 * HN_STR, ACT_STR = 144;
constexpr int L_RED = L_ACT + 8 * ACT_STR;
constexpr int L_Q = L_RED + 1152;
constexpr int L_FLG = L_Q + 512;
constexpr int L_END = L_FLG + 256;
static_assert(L_END <= 163840, "LDS");
constexpr int L_SCR = L_END;
constexpr int L_TOTAL = L_SCR + 2048 + 256;
static_assert(L_TOTAL <= 163840, "LDS");
constexpr int F_QREADY = 0, F_QDONE = 1, F_CHAIN = 2, F_TICK = 3, F_GOT = 4, F_XN = 5, F_FULL = 6, F_TAG = 8, F_S0 = 10, F_FIN = 12, F_END = 14;
#define CODE_PF 3
#define TLB_PF 1
#define EXP 0
#define MAPV 0
#define PACE 0
#define TAU 55
#define T_TARGET 3850
#define LEAD_F 60
#define LEAD_O 0
constexpr u64 TAG_STEP = 0x9E3779ull << 40 | 0x16Bull << 30 | 0x2D5ull << 20 | 0x0F3ull << 10 | 0x1B5ull;
#define TAG_FIELDS(t) v4u{(unsigned)(t) & 0x3ffu, ((unsigned)(t) >> 10) & 0x3ffu, ((unsigned)(t) >> 20) & 0x3ffu, (unsigned)((t) >> 30) & 0x3ffu}
#define WQ_AHEAD 12
#define ROT_ON 1
#define WINDOW 9
#define WIN() asm volatile("s_waitcnt vmcnt(" STR(WINDOW) ")" ::: "memory")
#define HP_DELAY 6
#define RP_DELAY 0
#define DONE_SLEEP 2
#define NHINT 2
#define RH_DELAY 16
#define DE 0
#define DE_DELAY 1
#define PHASE 1
#define XBASE xbase
#define ADAPT 1
#define WINDOW2 15
#define WIN2() do { } while (0)
#define KAUX 2
#define VAUX 2
#define WAUX 2
#define POL_0 ""
#define POL_2 "nt"
#define POL_3 "sc0 nt"
#define POL_18 "sc1 nt"
#define POL_19 "sc0 sc1 nt"
#define POL_1 "sc0"
#define POL_16 "sc1"
#define POL_17 "sc0 sc1"
#define POLCAT2(x) POL_##x
#define POLCAT(x) POLCAT2(x)
#define KPOL POLCAT(KAUX)
#define VPOL POLCAT(VAUX)
#define WPOL POLCAT(WAUX)
#define PPOLL_AUX AUX_SC1
#define QPOLL_AUX AUX_SC1
#define QPOLL_DELAY 50
#define MAXL 8
#define DMA_INFL 12
#define STR2(x) #x
#define STR(x) STR2(x)

constexpr int AUX_NT = 2, AUX_SC1 = 16, AUX_SC1NT = 18;
constexpr unsigned OOB = 0x7FFFFFF0u;
constexpr int SPIN_CAP = 1 << 17, LDS_SPIN_CAP = 1 << 22;
constexpr float LOG2E = 1.4426950408889634f;

#define SB() __builtin_amdgcn_sched_barrier(0)
#define CBAR() asm volatile("" ::: "memory")
#define WG_BARRIER() do { asm volatile("s_waitcnt lgkmcnt(0)\n\ts_barrier" ::: "memory"); } while (0)

__device__ __forceinline__ f32x4 mfma32(v4u a, v4u b, f32x4 c) {
  return __builtin_amdgcn_mfma_f32_16x16x32_bf16(__builtin_bit_cast(bf16x8, a), __builtin_bit_cast(bf16x8, b), c, 0, 0, 0);
}
__device__ __forceinline__ f32x4 mfma16(v2u a, v2u b, f32x4 c) {
  return __builtin_amdgcn_mfma_f32_16x16x16bf16_1k(__builtin_bit_cast(s16x4, a), __builtin_bit_cast(s16x4, b), c, 0, 0, 0);
}
__device__ __forceinline__ unsigned cvtpk(float lo, float hi) {
  f32x2_t v = {lo, hi};
  return __builtin_bit_cast(unsigned, __builtin_convertvector(v, bf16x2_t));
}
__device__ __forceinline__ float rbf(float x) { return __uint_as_float(cvtpk(x, x) & 0xffff0000u); }
__device__ __forceinline__ float bflo(unsigned x) { return __uint_as_float(x << 16); }
__device__ __forceinline__ float bfhi(unsigned x) { return __uint_as_float(x & 0xffff0000u); }
template <int CTRL> __device__ __forceinline__ float dppf(float x) {
  return __builtin_bit_cast(float, __builtin_amdgcn_mov_dpp(__builtin_bit_cast(int, x), CTRL, 0xf, 0xf, true));
}
template <int CTRL> __device__ __forceinline__ unsigned dppu(unsigned x) { return (unsigned)__builtin_amdgcn_mov_dpp((int)x, CTRL, 0xf, 0xf, true); }
constexpr int QP_XOR1 = 0xB1, QP_XOR2 = 0x4E, ROR4 = 0x124, ROR8 = 0x128, HALF_MIRROR = 0x141;
constexpr int QP_0011 = 0x50, QP_2233 = 0xFA, QP_0000 = 0x00, QP_1111 = 0x55, QP_ID = 0xE4;
__device__ __forceinline__ float xrow_max(float x) {
  auto s = __builtin_amdgcn_permlane16_swap(__float_as_uint(x), __float_as_uint(x), false, false);
  x = fmaxf(__uint_as_float(s[0]), __uint_as_float(s[1]));
  auto t = __builtin_amdgcn_permlane32_swap(__float_as_uint(x), __float_as_uint(x), false, false);
  return fmaxf(__uint_as_float(t[0]), __uint_as_float(t[1]));
}
__device__ __forceinline__ float xrow_sum(float x) {
  auto s = __builtin_amdgcn_permlane16_swap(__float_as_uint(x), __float_as_uint(x), false, false);
  x = __uint_as_float(s[0]) + __uint_as_float(s[1]);
  auto t = __builtin_amdgcn_permlane32_swap(__float_as_uint(x), __float_as_uint(x), false, false);
  return __uint_as_float(t[0]) + __uint_as_float(t[1]);
}
__device__ __forceinline__ unsigned xrow_xor(unsigned x) {
  auto s = __builtin_amdgcn_permlane16_swap(x, x, false, false);
  x = s[0] ^ s[1];
  auto t = __builtin_amdgcn_permlane32_swap(x, x, false, false);
  return t[0] ^ t[1];
}
__device__ __forceinline__ float row2_sum(float x) {
  auto s = __builtin_amdgcn_permlane16_swap(__float_as_uint(x), __float_as_uint(x), false, false);
  return __uint_as_float(s[0]) + __uint_as_float(s[1]);
}
__device__ __forceinline__ float wave_sum(float x) {
  x += dppf<QP_XOR1>(x); x += dppf<QP_XOR2>(x); x += dppf<ROR4>(x); x += dppf<ROR8>(x);
  return xrow_sum(x);
}
template <int SRC, int DST> __device__ __forceinline__ unsigned bank_mov(unsigned old, unsigned x) {
  constexpr int sh = ((DST - SRC) & 3) * 4;
  constexpr int ctrl = sh == 0 ? QP_ID : (0x120 + sh);
  return (unsigned)__builtin_amdgcn_update_dpp((int)old, (int)x, ctrl, 0xf, 1 << DST, false);
}
__device__ __forceinline__ rsrc_t mkrs(const void* p, unsigned bytes) { return __builtin_amdgcn_make_buffer_rsrc((void*)p, 0, bytes, 0x00020000); }
__device__ __forceinline__ v2u trread(LAS unsigned char* p) {
  return __builtin_bit_cast(v2u, __builtin_amdgcn_ds_read_tr16_b64_v4i16((LAS s16x4*)p));
}
__device__ __forceinline__ v4u trfrag(LAS unsigned char* p, int rowstride4) {
  const v2u a = trread(p), b = trread(p + rowstride4);
  return v4u{a.x, a.y, b.x, b.y};
}
__device__ __forceinline__ v4u tagged(f32x4 v, v4u t) {
  return v4u{(__float_as_uint(v.x) & ~0x3ffu) | t.x, (__float_as_uint(v.y) & ~0x3ffu) | t.y, (__float_as_uint(v.z) & ~0x3ffu) | t.z, (__float_as_uint(v.w) & ~0x3ffu) | t.w};
}
__device__ __forceinline__ void untag(v4u& v, v4u t, unsigned& bad) { v ^= t; bad |= (v.x | v.y) | (v.z | v.w); }
__device__ __forceinline__ void lds_wait_ge(volatile LAS unsigned* p, unsigned v) {
  for (int s = 0; *p < v && s < LDS_SPIN_CAP; ++s) __builtin_amdgcn_s_sleep(1);
  CBAR();
}
__device__ __forceinline__ void lds_spin_ge(volatile LAS unsigned* p, unsigned v) {
  for (int s = 0; *p < v && s < LDS_SPIN_CAP; ++s) { }
  CBAR();
}
__device__ __forceinline__ void ln_row(const float (&v)[12], bool live, v2u (&out)[3]) {
  const float ref = __uint_as_float(__builtin_amdgcn_readfirstlane(__float_as_uint(v[0])));
  float s1 = 0.f, s2 = 0.f;
#pragma unroll
  for (int i = 0; i < 12; ++i) { const float d = live ? v[i] - ref : 0.f; s1 += d; s2 = fmaf(d, d, s2); }
  s1 = wave_sum(s1); s2 = wave_sum(s2);
  const float md = s1 * (1.f / 576.f);
  const float var = fmaxf(fmaf(-md, md, s2 * (1.f / 576.f)), 0.f);
  const float m = rbf(ref + md);
  const float den = rbf(__builtin_amdgcn_sqrtf(rbf(rbf(var) + 1e-5f)));
  const float inv = __builtin_amdgcn_rcpf(den);
#pragma unroll
  for (int i = 0; i < 3; ++i) {
    const unsigned a = cvtpk(v[4 * i] - m, v[4 * i + 1] - m), c = cvtpk(v[4 * i + 2] - m, v[4 * i + 3] - m);
    out[i] = v2u{cvtpk(bflo(a) * inv, bfhi(a) * inv), cvtpk(bflo(c) * inv, bfhi(c) * inv)};
  }
}

#define STAMP(i) do { } while (0)

__global__ void __launch_bounds__(512, 2) fused_block(const u16* __restrict__ x, const u16* __restrict__ Kc, const u16* __restrict__ Vc, const u16* __restrict__ wq,
    const u16* __restrict__ wo, const u16* __restrict__ wgt, const u16* __restrict__ wup, const u16* __restrict__ wdn, u16* out, unsigned char* ws) {
  __shared__ __attribute__((aligned(16))) unsigned char smem_[L_TOTAL];
  LAS unsigned char* L = (LAS unsigned char*)smem_;
  volatile LAS unsigned* FLG = (volatile LAS unsigned*)(L + L_FLG);
  const int tid = threadIdx.x, lane = tid & 63, w = __builtin_amdgcn_readfirstlane(tid >> 6);
#define MKDESC(p, bytes) v4u{(unsigned)(u64)(p), (unsigned)((u64)(p) >> 32) & 0xffffu, (unsigned)(bytes), 0x00020000u}
  const unsigned lbase = (unsigned)(size_t)L;
#define DMA_ASMP(pol, desc, ldsa, voff, soff, imm) asm volatile("s_mov_b32 m0, %0\n\ts_nop 0\n\tbuffer_load_dwordx4 %1, %2, %3 offen offset:%4 " pol " lds" \
    :: "s"((ldsa) - (imm)), "v"(voff), "s"(desc), "s"(soff), "i"(imm) : "memory")
#define DMA_ASM(desc, ldsa, voff, soff, imm) DMA_ASMP(WPOL, desc, ldsa, voff, soff, imm)
  const bool live = lane < 48, two = (w == 2 || w == 7);
  const unsigned sw2 = (unsigned)((((lane & 7) ^ (((lane >> 4) & 1) << 1)) * 16) ^ (w << 6));
  v2u ep, xpk[3], xp2[3];
  if (w < 2) {
    {
      u64 pc; unsigned len;
      asm volatile("s_getpc_b64 %0\n.Lfb_code_begin:\n\ts_mov_b32 %1, .Lfb_code_end-.Lfb_code_begin" : "=s"(pc), "=s"(len));
      const v4u dc = MKDESC(pc, len);
      asm volatile("s_mov_b32 m0, %0\n\ts_nop 0\n\tbuffer_load_dword %1, %2, 0 offen lds\n\tbuffer_load_dword %3, %2, 0 offen lds"
        :: "s"(lbase + L_SCR + 2048), "v"(lane * 128 + w * 8192), "s"(dc), "v"(lane * 128 + w * 8192 + 16384) : "memory"); }
    const v4u dq = MKDESC(wq, 576 * 4096 * 2);
    const unsigned vo = (unsigned)((blockIdx.x & 3) * 144 + w * 8 + (lane >> 3)) * 8192u + (blockIdx.x >> 2) * 128 + sw2;
#pragma unroll
    for (int i = 0; i < 9; ++i) DMA_ASM(dq, lbase + L_WQ + w * 1024 + i * 2048, vo, i * 16 * 8192, 0);
    asm volatile("" : "=v"(ep), "=v"(xpk[0]), "=v"(xpk[1]), "=v"(xpk[2]), "=v"(xp2[0]), "=v"(xp2[1]), "=v"(xp2[2]));
  } else {
    const unsigned xo = (unsigned)(8 * (live ? lane : 47)), xo1 = xo + w * 1152, xo2 = xo + (w & 1) * 1152, z = 0;
    asm volatile("global_load_dwordx2 %0, %3, %4\n\tglobal_load_dwordx2 %1, %3, %4 offset:384\n\tglobal_load_dwordx2 %2, %3, %4 offset:768"
      : "=&v"(xpk[0]), "=&v"(xpk[1]), "=&v"(xpk[2]) : "v"(xo1), "s"(x) : "memory");
    asm volatile("global_load_dwordx2 %0, %1, %2 sc1" : "=&v"(ep) : "v"(z), "s"(ws) : "memory");
    if (two) asm volatile("global_load_dwordx2 %0, %3, %4\n\tglobal_load_dwordx2 %1, %3, %4 offset:384\n\tglobal_load_dwordx2 %2, %3, %4 offset:768"
      : "=&v"(xp2[0]), "=&v"(xp2[1]), "=&v"(xp2[2]) : "v"(xo2), "s"(x) : "memory");
    else asm volatile("" : "=v"(xp2[0]), "=v"(xp2[1]), "=v"(xp2[2]));
    if (w == 2) FLG[lane] = 0u;
  }
  WG_BARRIER();
  int wg = blockIdx.x;
  asm volatile("" : "+s"(wg));
  const int uu = MAPV == 0 ? wg : (MAPV == 1 ? (wg & 7) * 32 + (wg >> 3) : (MAPV == 2 ? ((wg * 37 + (wg >> 3) * 0) & 255) ^ ((wg >> 3) & 7) : (MAPV == 3 ? (wg ^ ((wg >> 3) & 7) ^ ((wg >> 6) & 3)) : (int)(__builtin_bitreverse32(wg) >> 24))));
  const int pair = uu >> 1, half = uu & 1, b = pair >> 4, kvh = pair & 15;
  const int rp = (pair & 7) * 16 + (pair >> 3);
  const int hsel = (pair >> 3) & 1;
  const bool holder = half == hsel;
  const bool isF = holder && rp < 72, isO = holder && rp >= 72 && rp < 108;
  const int fu = rp;
  const int sk = (rp - 72) & 3, sn = (rp - 72) >> 2;
  const int cb = wg >> 2, kq = wg & 3;
  const int n = lane & 15, g = lane >> 4, tq_ = (lane & 15) >> 2, tp = lane & 3;
  const int f2 = (tq_ >> 1) | ((g & 1) << 1);
  const rsrc_t rws = mkrs(ws, WS_END);
  const int dl = rp < 72 ? DELTA_F : (rp < 108 ? DELTA_O : 0);
  const int nt0 = 64 + (hsel ? dl : -dl);
  const int NT = half ? 128 - nt0 : nt0;
  const unsigned tsched = (unsigned)__builtin_amdgcn_s_memrealtime() + (unsigned)(T_TARGET - (isF ? LEAD_F : (isO ? LEAD_O : 0)) - NT * TAU);
  const unsigned tbase = ((unsigned)pair << 20) + (unsigned)(half * 64 + 64 - NT) * 8192u, xbase = ((unsigned)pair << 20) + (unsigned)((1 - half) * 64) * 8192u;
  const int voffK = (n >> 1) * 256 + (2 * g + (n & 1)) * 16;
  const int voffV = g * 2048 + n * 16;
  v4u kr[8], vr[8];
#define KOFF(i) (voffK + (((i) >> 1) & 1) * 2048 + ((i) & 1) * 128)
#define LDK(i, so) kr[i] = __builtin_amdgcn_raw_buffer_load_b128(rk, KOFF(i), (so) + ((i) >> 2) * 4096, KAUX)
#define LDV(i, so) vr[i] = __builtin_amdgcn_raw_buffer_load_b128(rv, voffV + (i) * 256, (so), VAUX)
  const rsrc_t rk = mkrs(Kc, 128u << 20), rv = mkrs(Vc, 128u << 20);
#define DMA_PACE() asm volatile("s_waitcnt vmcnt(" STR(DMA_INFL) ")" ::: "memory")
#define LDS_INC(f) do { if (lane == 0) __hip_atomic_fetch_add((LAS unsigned*)(FLG + (f)), 1u, __ATOMIC_RELAXED, __HIP_MEMORY_SCOPE_WORKGROUP); } while (0)
  const rsrc_t rx = mkrs(x, 8 * 576 * 2);
  u64 tag = 0;
  const unsigned guvo = (unsigned)(lane >> 3) * 9216u + fu * 128 + (((lane & 7) ^ ((lane >> 4) & 1)) * 16);
#define GU_DMA(i) do { const int mt_ = (i) >= 72, ri_ = (i) - 72 * mt_; const v4u dg_ = MKDESC(mt_ ? wup : wgt, 576 * 4608 * 2); \
    DMA_ASM(dg_, lbase + (i) * 1024, guvo ^ (unsigned)(((ri_ & 1) << 5) | (mt_ << 6)), ri_ * 8 * 9216, 0); } while (0)
  const int tl0 = isF ? N_EARLY * 1024 : (isO ? 131072 : 0);
  v4u de[8];
  const int dbk = w - 2;
#define DOWN_LOADS(arr, lo, hi) do { const rsrc_t rd = mkrs(wdn, dsz); \
    const unsigned vo_ = (dbk < 4 || n < 8) ? (unsigned)(fu * 64 + 8 * g) * 1152u + dbk * 256 + n * 16 : OOB; \
    _Pragma("unroll") for (int i = (lo); i < (hi); ++i) arr[i] = __builtin_amdgcn_raw_buffer_load_b128(rd, vo_, ((i >> 3) * 32 + (i & 7)) * 1152, WAUX); } while (0)
#define DOWN_TR(arr, o_) do { v4u t_[8]; \
      _Pragma("unroll") for (int jj = 0; jj < 8; ++jj) _Pragma("unroll") for (int mm = 0; mm < 4; ++mm) \
        t_[jj][mm] = __builtin_amdgcn_perm(arr[(o_) + 2 * mm + 1][jj >> 1], arr[(o_) + 2 * mm][jj >> 1], (jj & 1) ? 0x07060302u : 0x05040100u); \
      _Pragma("unroll") for (int jj = 0; jj < 8; ++jj) { arr[(o_) + jj] = t_[jj]; asm volatile("" : "+v"(arr[(o_) + jj])); } } while (0)

  const int nrounds = max((147456 - 4096 - tl0) >> 15, 0);
  if (w < 2) {
    static_assert(DMA_INFL == 12, "the wq signal sits behind the 12th piece");
    const int nw = isF ? N_EARLY : (isO ? 128 : 0);
#define LSTAMP() do { } while (0)
    LSTAMP();
#pragma unroll 1
    for (int run = 0; run < 2; ++run) {
      const int p0 = isF ? 72 * run : 64 * run, p1 = min(nw, isF ? p0 + 72 : p0 + 64);
      if (p1 <= p0) break;
      const v4u dw = isF ? MKDESC(run ? wup : wgt, 576 * 4608 * 2) : MKDESC(wo, 4096 * 576 * 2);
      const unsigned vo = isF ? (guvo ^ (unsigned)((w << 5) | (run << 6))) : (unsigned)(sk * 1024 + (lane >> 3)) * 1152u + sn * 128 + sw2;
      const unsigned sstep = isF ? 16u * 9216u : 16u * 1152u;
      unsigned so = (isF ? 0u : (unsigned)p0 * 8u * 1152u) + (sstep >> 1) * w, la = lbase + (p0 + w) * 1024;
      if (run) lds_wait_ge(FLG + F_QDONE, 4u);
#pragma unroll 1
      for (int i = p0; i < p1; i += 8) {
#pragma unroll
        for (int j = 0; j < 4; ++j) { DMA_ASM(dw, la, vo, so, 0); DMA_PACE(); la += 2048; so += sstep; }
        LSTAMP();
        if (i == 16) LDS_INC(F_XN);
      }
    }
    const v4u dk = MKDESC(Kc, 128u << 20), dv = MKDESC(Vc, 128u << 20);
#pragma unroll 1
    for (int rd = 0; rd < nrounds; ++rd) {
      const int slot = 2 * rd + w;
      if (tl0 + 4096 + (slot + 1) * 16384 > L_WQ) lds_wait_ge(FLG + F_QDONE, 4u);
      const unsigned so = ((PHASE && slot < NT - 64) ? XBASE : tbase) + (unsigned)slot * 8192u, la = lbase + tl0 + 4096 + slot * 16384;
#pragma unroll
      for (int i = 0; i < 8; ++i) { DMA_ASMP(KPOL, dk, la + i * 1024, voffK, so + (i >> 2) * 4096, ((i >> 1) & 1) * 2048 + (i & 1) * 128); DMA_PACE(); if ((i & 3) == 3) LSTAMP(); }
#pragma unroll
      for (int i = 0; i < 8; ++i) { DMA_ASMP(VPOL, dv, la + 8192 + i * 1024, voffV, so, i * 256); DMA_PACE(); if ((i & 3) == 3) LSTAMP();
        if (i == 3 && rd == 0 && nw == 0) LDS_INC(F_XN); }
      FLG[F_FULL + w] = rd;
    }
    SB();
#pragma unroll
    for (int i = 0; i < 8; ++i) { LDK(i, tbase + (2 * nrounds + w) * 8192); SB(); DMA_PACE(); if ((i & 3) == 3) LSTAMP(); }
#pragma unroll
    for (int i = 0; i < 8; ++i) { LDV(i, tbase + (2 * nrounds + w) * 8192); SB(); DMA_PACE(); if ((i & 3) == 3) LSTAMP(); }
    FLG[F_FULL + w] = nrounds;
  } else {
    if (two) asm volatile("s_waitcnt vmcnt(4)" ::: "memory"); else asm volatile("s_waitcnt vmcnt(1)" ::: "memory");
    asm volatile("" : "+v"(xpk[0]), "+v"(xpk[1]), "+v"(xpk[2]));
#define LN_TO_XN(src, row) do { float v[12]; \
      _Pragma("unroll") for (int i = 0; i < 3; ++i) { v[4 * i] = bflo(src[i].x); v[4 * i + 1] = bfhi(src[i].x); v[4 * i + 2] = bflo(src[i].y); v[4 * i + 3] = bfhi(src[i].y); } \
      v2u o3[3]; ln_row(v, live, o3); \
      if (live) { _Pragma("unroll") for (int i = 0; i < 3; ++i) *(LAS v2u*)(L + L_XN + (row) * XN_STR + (192 * i + 4 * lane) * 2) = o3[i]; } } while (0)
    LN_TO_XN(xpk, w);
    SB();
    asm volatile("s_waitcnt vmcnt(0)" : "+v"(ep), "+v"(xp2[0]), "+v"(xp2[1]), "+v"(xp2[2]) :: "memory");
    if (two) LN_TO_XN(xp2, w & 1);
    asm volatile("s_waitcnt lgkmcnt(0)" ::: "memory");
    LDS_INC(F_XN);
    tag = ((u64)(unsigned)__builtin_amdgcn_readfirstlane(ep.y) << 32 | (unsigned)__builtin_amdgcn_readfirstlane(ep.x)) + TAG_STEP;
    const v4u tg = TAG_FIELDS(tag);
    if (w == 7) { FLG[F_TAG] = (unsigned)tag; FLG[F_TAG + 1] = (unsigned)(tag >> 32); }
    const v4u tq4 = tg & 15u;
    if (!two) {
      lds_spin_ge(FLG + F_XN, 8u);
      const int tq = w - 3;
      v4u a[5], bb[5];
#pragma unroll
      for (int ks = 0; ks < 5; ++ks) {
        const int gg = ks < 4 ? g : (g & 1);
        a[ks] = trfrag(L + L_WQ + (32 * ks + 8 * gg + tq_) * 128 + ((tq ^ f2) * 32) + tp * 8, 4 * 128);
        bb[ks] = *(LAS v4u*)(L + L_XN + (n & 7) * XN_STR + (kq * 144 + 32 * ks + 8 * gg) * 2);
      }
      SB();
      if (g >= 2) bb[4] = v4u{0, 0, 0, 0};
      f32x4 d = {0.f, 0.f, 0.f, 0.f};
#pragma unroll
      for (int ks = 0; ks < 5; ++ks) d = mfma32(a[ks], bb[ks], d);
      __builtin_amdgcn_raw_buffer_store_b128((__builtin_bit_cast(v4u, d) & ~15u) | tq4, rws, n < 8 ? WS_QG + ((n * 4 + kq) * 4096 + cb * 64 + tq * 16 + 4 * g) * 4 : OOB, 0, AUX_SC1);
      LDS_INC(F_QDONE);
      STAMP(2);
    }
    if (w == 7) {
      const int qo = WS_QG + (b * 4 * 4096 + kvh * 256 + 4 * lane) * 4;
      v4u a[4];
      if (QPOLL_DELAY) __builtin_amdgcn_s_sleep(QPOLL_DELAY);
#pragma unroll 1
      for (int s = 0; s < SPIN_CAP; ++s) {
#pragma unroll
        for (int i = 0; i < 4; ++i) a[i] = __builtin_amdgcn_raw_buffer_load_b128(rws, qo, i * 16384, QPOLL_AUX);
        unsigned bad = 0;
#pragma unroll
        for (int i = 0; i < 4; ++i) untag(a[i], tq4, bad);
        bad |= dppu<QP_XOR1>(bad); bad |= dppu<QP_XOR2>(bad);
        if ((bad & 15u) == 0u) break;
        __builtin_amdgcn_s_sleep(2);
        CBAR();
      }
#define QV(p) __uint_as_float((p) | 8u)
#define QS(c) ((QV(a[0].c) + QV(a[1].c)) + (QV(a[2].c) + QV(a[3].c)))
      *(LAS v2u*)(L + L_Q + lane * 8) = v2u{cvtpk(QS(x), QS(y)), cvtpk(QS(z), QS(w))};
      asm volatile("s_waitcnt lgkmcnt(0)" ::: "memory");
      FLG[F_QREADY] = 1u;
      STAMP(2);
      {
        unsigned j0 = __builtin_amdgcn_raw_buffer_load_b32(rws, lane < 2 ? WS_O + lane * (1200 << 10) : OOB, 0, AUX_NT);
        unsigned j1 = __builtin_amdgcn_raw_buffer_load_b32(mkrs(out, 8 * 576 * 2), lane == 0 ? 0 : OOB, 0, AUX_NT);
        unsigned j2 = __builtin_amdgcn_raw_buffer_load_b32(mkrs(wdn, 4608 * 576 * 2), (isF && lane < 2) ? (fu * 64 + lane * 63) * 1152 : OOB, 0, AUX_NT);
        asm volatile("" :: "v"(j0), "v"(j1), "v"(j2)); }
    }
#pragma unroll
    for (int i = 0; i < 8; ++i) asm volatile("" : "=v"(kr[i]), "=v"(vr[i]));
  }
  if (w != 7) {
    lds_wait_ge(FLG + F_QREADY, 1u);
    STAMP(4);
    v4u qf[8];
    { const v4u qa0 = *(LAS v4u*)(L + L_Q + (((n >> 1) & 1) * 128 + 16 * g + 8 * (n & 1)) * 2), qa1 = *(LAS v4u*)(L + L_Q + (((n >> 1) & 1) * 128 + 64 + 16 * g + 8 * (n & 1)) * 2);
#pragma unroll
      for (int i = 0; i < 8; ++i) qf[i] = ((n >> 2) == (i >> 1)) ? ((i & 1) ? qa1 : qa0) : v4u{0, 0, 0, 0}; }
    const bool par = lane & 1;
    const unsigned jbmask = par ? 0xffff0000u : 0x0000ffffu;
    float m = -INFINITY, l = 0.f;
    f32x4 o = {0.f, 0.f, 0.f, 0.f};
    v4u pa0 = {0, 0, 0, 0}, pa1 = {0, 0, 0, 0};
#define PV(i, pa, xs_, MORE) { pa.x = bank_mov<((i) >> 1), 0>(pa.x, xs_); pa.y = bank_mov<((i) >> 1), 1>(pa.y, xs_); pa.z = bank_mov<((i) >> 1), 2>(pa.z, xs_); pa.w = bank_mov<((i) >> 1), 3>(pa.w, xs_); \
        o = mfma32(pa, vr[i], o); if (MORE) { WINA(); LDV(i, so); } SB(); }
#define BODY(MORE) { \
      f32x4 d = {0.f, 0.f, 0.f, 0.f}; \
      _Pragma("unroll") for (int i = 0; i < 8; ++i) { d = mfma32(qf[i], kr[i], d); if (MORE) { WINA(); LDK(i, so); } SB(); } \
      const float keep = par ? d.w : d.x, send = par ? d.y : d.z; \
      float s = keep + dppf<QP_XOR1>(send); \
      s = rbf(rbf(s) * 0.08838834764831845f); \
      if (__any(s > m)) { \
        float t = s; t = fmaxf(t, dppf<QP_XOR2>(t)); t = fmaxf(t, dppf<ROR4>(t)); t = fmaxf(t, dppf<ROR8>(t)); t = xrow_max(t); \
        const float mn = fmaxf(m, t); \
        const float al = __builtin_amdgcn_exp2f((m - mn) * LOG2E); \
        m = mn; l *= al; \
        const float a0 = dppf<QP_0000>(al), a1 = dppf<QP_1111>(al); \
        o.x *= a0; o.y *= a0; o.z *= a1; o.w *= a1; \
      } \
      const float p = __builtin_amdgcn_exp2f((s - m) * LOG2E); \
      l += p; \
      const unsigned p2 = cvtpk(p, p); \
      const unsigned x0 = dppu<QP_0011>(p2) & jbmask, x1 = dppu<QP_2233>(p2) & jbmask; \
      PV(0, pa0, x0, MORE) PV(1, pa1, x1, MORE) PV(2, pa0, x0, MORE) PV(3, pa1, x1, MORE) PV(4, pa0, x0, MORE) PV(5, pa1, x1, MORE) PV(6, pa0, x0, MORE) PV(7, pa1, x1, MORE) }
    if (w < 2) {
#pragma unroll 1
      for (int tnext = 2 * nrounds + 2 + w; tnext < NT; tnext += 2) { const unsigned so = tbase + (unsigned)tnext * 8192u;
        const int ahead = (int)((unsigned)__builtin_amdgcn_s_memrealtime() - tsched - (unsigned)(tnext * TAU));
#define WINA() do { WIN2(); asm volatile("s_cmp_ge_i32 %0, 0\n\ts_cbranch_scc1 1f\n\ts_waitcnt vmcnt(" STR(WINDOW) ")\n1:" :: "s"(ahead) : "memory", "scc"); } while (0)
        BODY(1)
      }
    }
#define REC_O(r) (*(LAS f32x4*)(L + L_REC + (r) * REC_STR + lane * 16))
#define REC_ML(r) (*(LAS f32x2_t*)(L + L_REC + (r) * REC_STR + 1024 + lane * 8))
#define MERGE(ro, rml) do { const float mn_ = fmaxf(m, rml.x), ms_ = mn_ == -INFINITY ? 0.f : mn_; \
      const float ea_ = __builtin_amdgcn_exp2f((m - ms_) * LOG2E), eb_ = __builtin_amdgcn_exp2f((rml.x - ms_) * LOG2E); \
      m = mn_; l = __fadd_rn(__fmul_rn(l, ea_), __fmul_rn(rml.y, eb_)); \
      const float a0_ = dppf<QP_0000>(ea_), a1_ = dppf<QP_1111>(ea_), b0_ = dppf<QP_0000>(eb_), b1_ = dppf<QP_1111>(eb_); \
      o.x = __fadd_rn(__fmul_rn(o.x, a0_), __fmul_rn(ro.x, b0_)); o.y = __fadd_rn(__fmul_rn(o.y, a0_), __fmul_rn(ro.y, b0_)); \
      o.z = __fadd_rn(__fmul_rn(o.z, a1_), __fmul_rn(ro.z, b1_)); o.w = __fadd_rn(__fmul_rn(o.w, a1_), __fmul_rn(ro.w, b1_)); } while (0)
    if (w == 0 && nrounds > 0) {
      lds_wait_ge(FLG + F_CHAIN, 5u);
      const f32x4 ro = REC_O(2); const f32x2_t rml = REC_ML(2); MERGE(ro, rml);
    }
    const u64 tagp = (u64)(unsigned)__builtin_amdgcn_readfirstlane(FLG[F_TAG + 1]) << 32 | (unsigned)__builtin_amdgcn_readfirstlane(FLG[F_TAG]);
#pragma unroll 1
    for (int t = w < 2 ? -1 : w - 2; t < 2 * nrounds; t += 5) {
      if (t >= 0) {
        lds_wait_ge(FLG + F_FULL + (t & 1), (unsigned)((t >> 1) + 1));
#pragma unroll
        for (int i = 0; i < 8; ++i) { kr[i] = *(LAS v4u*)(L + tl0 + 4096 + t * 16384 + i * 1024 + lane * 16); vr[i] = *(LAS v4u*)(L + tl0 + 4096 + t * 16384 + 8192 + i * 1024 + lane * 16); }
      }
      const unsigned so = 0; const int ahead = 0; BODY(0)
      if (t < 0) break;
    }
    STAMP(5);
    bool pub = true;
    if (w >= 2) {
      if (w == 2) lds_wait_ge(FLG + F_QDONE, 4u);
      else lds_wait_ge(FLG + F_CHAIN, (unsigned)(w - 2));
    } else {
      if (FLG[F_TICK] == 0u) {
        REC_O(w) = o; REC_ML(w) = f32x2_t{m, l};
        asm volatile("s_waitcnt lgkmcnt(0)" ::: "memory");
        unsigned tk = 0;
        if (lane == 0) tk = __hip_atomic_fetch_add((LAS unsigned*)(FLG + F_TICK), 1u, __ATOMIC_RELAXED, __HIP_MEMORY_SCOPE_WORKGROUP);
        pub = __builtin_amdgcn_readfirstlane(tk) != 0u;
      }
      CBAR();
    }
    if (pub) {
      if (w != 2) { const int r = w < 2 ? 1 - w : 2; const f32x4 ro = REC_O(r); const f32x2_t rml = REC_ML(r); MERGE(ro, rml); }
      if (w >= 2) { REC_O(2) = o; REC_ML(2) = f32x2_t{m, l}; asm volatile("s_waitcnt lgkmcnt(0)" ::: "memory"); FLG[F_CHAIN] = w - 1; }
      const rsrc_t rp_ = mkrs(ws, w < 2 ? WS_END : 0);
      l += dppf<QP_XOR2>(l); l += dppf<ROR4>(l); l += dppf<ROR8>(l); l = xrow_sum(l);
      const float inv = __builtin_amdgcn_rcpf(l), i0 = dppf<QP_0000>(inv), i1 = dppf<QP_1111>(inv);
      const int slot = kvh * 16 + b * 2 + half;
      const v2u pk = {cvtpk(o.x * i0, o.y * i0), cvtpk(o.z * i1, o.w * i1)};
      __builtin_amdgcn_raw_buffer_store_b64(pk, rp_, WS_O + kvh * 8192 + ((n >> 2) * 2 + (g >> 1)) * OSEG + (b * 2 + half) * OROW + (n & 3) * 16 + (g & 1) * 8, 0, AUX_SC1);
      unsigned ck = pk.x ^ pk.y;
      ck ^= dppu<QP_XOR1>(ck); ck ^= dppu<QP_XOR2>(ck); ck ^= dppu<HALF_MIRROR>(ck); ck = xrow_xor(ck);
      const unsigned lse0 = __float_as_uint(fmaf(m, LOG2E, __builtin_amdgcn_logf(l))), lse1 = dppu<QP_1111>(lse0);
      const unsigned salt = lse0 ^ __builtin_rotateleft32(lse1, 13) ^ (unsigned)(tagp >> 32);
      __builtin_amdgcn_raw_buffer_store_b128(v4u{lse0, lse1, ck ^ salt, dppu<ROR8>(ck) ^ salt}, rp_, lane == 0 ? WS_LSE + slot * LSE_STR : OOB, 0, AUX_SC1);
    }
  }
  if (DE > 0) {
    if (isF && w >= 2 && w < 7) { const unsigned dsz = 4608 * 576 * 2; for (int z = 0; z < DE_DELAY * (w - 1); ++z) __builtin_amdgcn_s_sleep(127); DOWN_LOADS(de, 0, DE);
      if (DE == 8) DOWN_TR(de, 0);
#pragma unroll
      for (int i = 0; i < DE; ++i) asm volatile("" : "+v"(de[i])); }
    else {
#pragma unroll
      for (int i = 0; i < DE; ++i) asm volatile("" : "=v"(de[i])); }
  }
#pragma unroll 1
  for (int real = (w == 7) ? 0 : 1; real < 2; ++real) {
  const rsrc_t rt = mkrs(ws, real ? WS_END : 0);
  int lane_ = tid & 63;
  asm volatile("" : "+v"(lane_));
  const int lane = lane_, n = lane & 15, g = lane >> 4, tq_ = (lane & 15) >> 2, tp = lane & 3, f2 = (tq_ >> 1) | ((g & 1) << 1);
  const bool live = lane < 48;
  if (real) WG_BARRIER();
  STAMP(7);
  tag = real ? ((u64)(unsigned)__builtin_amdgcn_readfirstlane(FLG[F_TAG + 1]) << 32 | (unsigned)__builtin_amdgcn_readfirstlane(FLG[F_TAG])) : 0ull;
  const v4u tg = TAG_FIELDS(tag);
  const unsigned thint = (unsigned)(tag >> 32);
  if (isO) {
    v4u af[16];
#pragma unroll
    for (int i = 0; i < 16; ++i) af[i] = trfrag(L + L_WO + ((w >> 1) * 256 + (i >> 3) * 128 + (w & 1) * 64 + ((i >> 2) & 1) * 32 + 8 * g + tq_) * 128 + (((i & 3) ^ f2) * 32) + tp * 8, 4 * 128);
    const int kv_ = sk * 4 + (w >> 1);
    v4u of[4], ls;
#pragma unroll 1
    for (int s = 0; s < SPIN_CAP; ++s) {
      ls = __builtin_amdgcn_raw_buffer_load_b128(rt, WS_LSE + (kv_ * 16 + n) * LSE_STR, 0, AUX_SC1);
#pragma unroll
      for (int i = 0; i < 4; ++i) of[i] = __builtin_amdgcn_raw_buffer_load_b128(rt, WS_O + kv_ * 8192 + (w & 1) * 4 * OSEG + n * OROW + g * 16 + i * OSEG, 0, AUX_SC1);
      const v4u xx = (of[0] ^ of[1]) ^ (of[2] ^ of[3]);
      const unsigned ck = xrow_xor((xx.x ^ xx.y) ^ (xx.z ^ xx.w)) ^ ls.x ^ __builtin_rotateleft32(ls.y, 13) ^ thint ^ ((w & 1) ? ls.w : ls.z);
      if (ck == 0u) break;
      __builtin_amdgcn_s_sleep(1);
      CBAR();
    }
    { unsigned tk = 0;
      if (lane == 0 && real) tk = __hip_atomic_fetch_add((LAS unsigned*)(FLG + F_GOT), 1u, __ATOMIC_RELAXED, __HIP_MEMORY_SCOPE_WORKGROUP);
      if (sn == 0 && (__builtin_amdgcn_readfirstlane(tk) == 7u || !real)) {
#pragma unroll
        for (int i = 0; i < NHINT; ++i) __builtin_amdgcn_raw_buffer_store_b32(thint, rt, WS_DONE + lane * 128 + sk * 4, i * 8192, AUX_SC1); } }
    STAMP(9);
#define U2F __uint_as_float
    f32x4 d[8];
#pragma unroll
    for (int t = 0; t < 8; ++t) d[t] = f32x4{0.f, 0.f, 0.f, 0.f};
#pragma unroll
    for (int ks = 0; ks < 2; ++ks) {
      const v4u b0 = {of[2 * ks].x, of[2 * ks].z, of[2 * ks + 1].x, of[2 * ks + 1].z}, b1 = {of[2 * ks].y, of[2 * ks].w, of[2 * ks + 1].y, of[2 * ks + 1].w};
#pragma unroll
      for (int t = 0; t < 4; ++t) { d[t] = mfma32(af[ks * 4 + t], b0, d[t]); d[4 + t] = mfma32(af[8 + ks * 4 + t], b1, d[4 + t]); }
    }
    const float e0 = U2F(ls.x), e1 = U2F(ls.y);
    const float w0 = __builtin_amdgcn_rcpf(1.f + __builtin_amdgcn_exp2f(dppf<QP_XOR1>(e0) - e0)), w1 = __builtin_amdgcn_rcpf(1.f + __builtin_amdgcn_exp2f(dppf<QP_XOR1>(e1) - e1));
    {
      const bool od = n & 1;
      f32x4 r[4];
#pragma unroll
      for (int t = 0; t < 4; ++t) r[t] = w0 * d[t] + w1 * d[4 + t];
      f32x4 k0 = od ? r[2] : r[0], k1 = od ? r[3] : r[1], s0 = od ? r[0] : r[2], s1 = od ? r[1] : r[3];
      k0.x += dppf<QP_XOR1>(s0.x); k0.y += dppf<QP_XOR1>(s0.y); k0.z += dppf<QP_XOR1>(s0.z); k0.w += dppf<QP_XOR1>(s0.w);
      k1.x += dppf<QP_XOR1>(s1.x); k1.y += dppf<QP_XOR1>(s1.y); k1.z += dppf<QP_XOR1>(s1.z); k1.w += dppf<QP_XOR1>(s1.w);
      LAS unsigned char* dst = L + (real ? L_RED1 + w * 2048 : L_SCR) + (n >> 1) * 256 + (od ? 128 : 0) + g * 16;
      *(LAS f32x4*)dst = k0; *(LAS f32x4*)(dst + 64) = k1; }
    if (real) WG_BARRIER();
    {
      f32x4 t = *(LAS f32x4*)(L + L_RED1 + g * 4096 + w * 256 + n * 16) + *(LAS f32x4*)(L + L_RED1 + g * 4096 + 2048 + w * 256 + n * 16);
      t.x = xrow_sum(t.x); t.y = xrow_sum(t.y); t.z = xrow_sum(t.z); t.w = xrow_sum(t.w);
      __builtin_amdgcn_raw_buffer_store_b128(tagged(t, tg), rt, lane < 16 ? WS_H + ((w * 4 + sk) * 576 + sn * 64 + n * 4) * 4 : OOB, 0, AUX_SC1);
      STAMP(10);
    }
  } else if (isF) {
    v4u df[16];
#pragma unroll
    for (int i = 0; i < DE; ++i) df[i] = de[i];
    const bool dwave = (w >= 2 && w < 7) || !real;
    v2u xpk[3];
#pragma unroll
    for (int i = 0; i < 3; ++i) xpk[i] = __builtin_amdgcn_raw_buffer_load_b64(rx, 8 * (live ? lane : 47) + 384 * i, w * 1152, 0);
    SB();
    if (N_EARLY < 144 && real) {
#pragma unroll 1
      for (int i = N_EARLY + w; i < 144; i += 8) GU_DMA(i);
    }
    static_assert(N_EARLY == 144, "");
    if (dwave) { const unsigned dsz = real ? 4608 * 576 * 2 : 0; DOWN_LOADS(df, DE, 16); SB(); }
    else {
#pragma unroll
      for (int i = DE; i < 16; ++i) asm volatile("" : "=v"(df[i]));
    }
    if (N_EARLY < 144 && real) WG_BARRIER();
    SB();
    v4u gf[18];
    { LAS unsigned char* ga = L + ((tp & 1) ? L_UP : L_GATE) + (8 * g + tq_) * 128 + ((w ^ (f2 | ((tp & 1) << 2))) * 16) + (tp >> 1) * 8;
#pragma unroll
      for (int ks = 0; ks < 18; ++ks) gf[ks] = trfrag(ga + ks * 32 * 128, 4 * 128); }
#pragma unroll
    for (int ks = 0; ks < 18; ++ks) asm volatile("" : "+v"(gf[ks]));
    if (dwave) { if (DE != 8) DOWN_TR(df, 0); DOWN_TR(df, 8); }
    asm volatile("" : "+v"(xpk[0]), "+v"(xpk[1]), "+v"(xpk[2]));
    STAMP(6);
#define HINT_WAIT(idx) do { _Pragma("unroll 1") for (int s = 0; s < SPIN_CAP; ++s) { \
        const unsigned f0 = __builtin_amdgcn_raw_buffer_load_b32(rt, WS_DONE + (idx) * 128 + (lane & 3) * 4, 0, AUX_SC1); \
        if (f0 == thint) break; \
        __builtin_amdgcn_s_sleep(DONE_SLEEP); CBAR(); } } while (0)
    if (w == 7) { HINT_WAIT(fu); if (HP_DELAY) __builtin_amdgcn_s_sleep(HP_DELAY); }
    if (real) WG_BARRIER();
    int hoff[3];
#pragma unroll
    for (int i = 0; i < 3; ++i) { const int c = 24 * i + (lane >> 1); hoff[i] = (c & 3) * 288 + (c >> 2) * 16 + (lane & 1) * 8; asm volatile("" : "+v"(hoff[i])); }
    v4u hp[12];
#pragma unroll
    for (int i = 0; i < 12; ++i) asm volatile("" : "=v"(hp[i]));
    STAMP(3);
    unsigned pend = 15u;
#pragma unroll 1
    for (int s = 0; s < SPIN_CAP; ++s) {
#pragma unroll
      for (int q = 0; q < 4; ++q) if ((pend >> q) & 1u) {
#pragma unroll
        for (int i = 3 * q; i < 3 * q + 3; ++i) hp[i] = __builtin_amdgcn_raw_buffer_load_b128(rt, WS_H + (w * 4 * 576 + 4 * (live ? lane : 47)) * 4, i * 768, AUX_SC1); }
      SB();
#pragma unroll
      for (int q = 0; q < 4; ++q) if ((pend >> q) & 1u) {
        unsigned bad = 0;
#pragma unroll
        for (int i = 3 * q; i < 3 * q + 3; ++i) untag(hp[i], tg, bad);
        if (!__any((bad & 0x3ffu) != 0u)) pend &= ~(1u << q); }
      if (pend == 0u) break;
      CBAR();
    }
    STAMP(9);
    { float v[12];
#pragma unroll
      for (int i = 0; i < 3; ++i) {
        const f32x4 a = (__builtin_bit_cast(f32x4, hp[i]) + __builtin_bit_cast(f32x4, hp[3 + i])) + (__builtin_bit_cast(f32x4, hp[6 + i]) + __builtin_bit_cast(f32x4, hp[9 + i]));
        const unsigned r0 = cvtpk(a.x, a.y), r1 = cvtpk(a.z, a.w);
        const unsigned h0 = cvtpk(bflo(xpk[i].x) + bflo(r0), bfhi(xpk[i].x) + bfhi(r0)), h1 = cvtpk(bflo(xpk[i].y) + bflo(r1), bfhi(xpk[i].y) + bfhi(r1));
        v[4 * i] = bflo(h0); v[4 * i + 1] = bfhi(h0); v[4 * i + 2] = bflo(h1); v[4 * i + 3] = bfhi(h1); }
      v2u o3[3]; ln_row(v, live, o3);
      if (live) {
#pragma unroll
        for (int i = 0; i < 3; ++i) *(LAS v2u*)(L + (real ? L_XN + w * HN_STR : L_SCR) + hoff[i]) = o3[i]; } }
    if (real) WG_BARRIER();
    STAMP(11);
    {
      v4u hb[9];
#pragma unroll
      for (int ks = 0; ks < 9; ++ks) hb[ks] = *(LAS v4u*)(L + L_XN + (n & 7) * HN_STR + g * 288 + (n >> 3) * 144 + ks * 16);
      SB();
      f32x4 d = {0.f, 0.f, 0.f, 0.f}, d2 = {0.f, 0.f, 0.f, 0.f};
#pragma unroll
      for (int ks = 0; ks < 9; ++ks) { d = mfma32(gf[ks], hb[ks], d); d2 = mfma32(gf[9 + ks], hb[ks], d2); }
      d.x += dppf<ROR8>(d2.x); d.y += dppf<ROR8>(d2.y); d.z += dppf<ROR8>(d2.z); d.w += dppf<ROR8>(d2.w);
      auto s0 = __builtin_amdgcn_permlane16_swap(__float_as_uint(d.x), __float_as_uint(d.z), false, false);
      auto s1 = __builtin_amdgcn_permlane16_swap(__float_as_uint(d.y), __float_as_uint(d.w), false, false);
      float av[2];
#pragma unroll
      for (int e = 0; e < 2; ++e) {
        const float gg = rbf(__uint_as_float(e ? s1[0] : s0[0])), uu = rbf(__uint_as_float(e ? s1[1] : s0[1]));
        const float z2 = gg * fmaf(gg * gg, 0.044715f, 1.f) * (2.f * 0.7978845608028654f * LOG2E);
        const float ge = rbf(gg * __builtin_amdgcn_rcpf(1.f + __builtin_amdgcn_exp2f(-z2)));
        av[e] = ge * uu; }
      unsigned pk = cvtpk(av[0], av[1]);
      asm volatile("" : "+v"(pk));
      if (n < 8) *(LAS unsigned*)(L + L_ACT + n * ACT_STR + (8 * w + 2 * g) * 2) = pk; }
    if (real) WG_BARRIER();
    STAMP(12);
    if (dwave) {
      const v4u b0 = *(LAS v4u*)(L + L_ACT + (n & 7) * ACT_STR + g * 16), b1 = *(LAS v4u*)(L + L_ACT + (n & 7) * ACT_STR + 64 + g * 16);
      f32x4 d[8];
#pragma unroll
      for (int jj = 0; jj < 8; ++jj) d[jj] = mfma32(df[jj], b0, f32x4{0.f, 0.f, 0.f, 0.f});
#pragma unroll
      for (int jj = 0; jj < 8; ++jj) d[jj] = mfma32(df[8 + jj], b1, d[jj]);
      const bool hq = n >= 8;
      const unsigned po = (dbk < 4 || g < 2) ? WS_P + ((32 * dbk + 8 * g + (n >> 3)) * 72 + fu) * 128 + (n & 7) * 16 : OOB;
#pragma unroll
      for (int e = 0; e < 4; ++e)
        __builtin_amdgcn_raw_buffer_store_b128(tagged(f32x4{hq ? d[4][e] : d[0][e], hq ? d[5][e] : d[1][e], hq ? d[6][e] : d[2][e], hq ? d[7][e] : d[3][e]}, tg), rt, po, 2 * e * 72 * 128, AUX_SC1);
    }
  } else {
    const int unit = rp < 108 ? rp : 108 + (rp - 108) * 2 + half;
    if (unit < 144) {
      const int ob_ = lane & 7, oc = unit * 4;
      v2u xo = {0, 0};
      f32x4 a = {0.f, 0.f, 0.f, 0.f};
      if (w == 7) {
        xo = *(const GAS v2u*)(x + ob_ * 576 + oc);
        v4u hv;
        if (NHINT == 4) { HINT_WAIT(72 + unit); __builtin_amdgcn_s_sleep(RH_DELAY); }
#pragma unroll 1
        for (int s = 0; s < SPIN_CAP; ++s) {
          hv = __builtin_amdgcn_raw_buffer_load_b128(rt, WS_H + ((ob_ * 4 + ((lane >> 3) & 3)) * 576 + oc) * 4, 0, AUX_SC1);
          unsigned bad = 0; untag(hv, tg, bad);
          if ((bad & 0x3ffu) == 0u) break;
          __builtin_amdgcn_s_sleep(NHINT == 4 ? 4 : 16);
          CBAR();
        }
        a = __builtin_bit_cast(f32x4, hv);
        a.x += dppf<ROR8>(a.x); a.y += dppf<ROR8>(a.y); a.z += dppf<ROR8>(a.z); a.w += dppf<ROR8>(a.w);
        a.x = row2_sum(a.x); a.y = row2_sum(a.y); a.z = row2_sum(a.z); a.w = row2_sum(a.w);
        if (RP_DELAY) __builtin_amdgcn_s_sleep(RP_DELAY);
      }
      if (real) WG_BARRIER();
      STAMP(9);
      v4u p0, p1;
#pragma unroll 1
      for (int s = 0; s < SPIN_CAP; ++s) {
        p0 = __builtin_amdgcn_raw_buffer_load_b128(rt, WS_P + unit * 9216 + w * 1024 + lane * 16, 0, PPOLL_AUX);
        p1 = __builtin_amdgcn_raw_buffer_load_b128(rt, WS_P + unit * 9216 + 8192 + w * 128 + (lane & 7) * 16, 0, PPOLL_AUX);
        unsigned bad = 0; untag(p0, tg, bad); untag(p1, tg, bad);
        if ((bad & 0x3ffu) == 0u) break;
        __builtin_amdgcn_s_sleep(1);
        CBAR();
      }
      STAMP(11);
      f32x4 t = __builtin_bit_cast(f32x4, p0);
      t.x += dppf<ROR8>(t.x); t.y += dppf<ROR8>(t.y); t.z += dppf<ROR8>(t.z); t.w += dppf<ROR8>(t.w);
      t.x = xrow_sum(t.x); t.y = xrow_sum(t.y); t.z = xrow_sum(t.z); t.w = xrow_sum(t.w);
      if (lane < 8) *(LAS f32x4*)(L + L_RED + w * 128 + lane * 16) = t + __builtin_bit_cast(f32x4, p1);
      if (real) WG_BARRIER();
      if (w == 7 && lane < 8) {
        f32x4 f = *(LAS f32x4*)(L + L_RED + lane * 16);
#pragma unroll
        for (int r = 1; r < 8; ++r) f += *(LAS f32x4*)(L + L_RED + r * 128 + lane * 16);
        const unsigned r0 = cvtpk(a.x, a.y), r1 = cvtpk(a.z, a.w);
        const unsigned hh0 = cvtpk(bflo(xo.x) + bflo(r0), bfhi(xo.x) + bfhi(r0)), hh1 = cvtpk(bflo(xo.y) + bflo(r1), bfhi(xo.y) + bfhi(r1));
        const unsigned f0 = cvtpk(f.x, f.y), f1 = cvtpk(f.z, f.w);
        __builtin_amdgcn_raw_buffer_store_b64(v2u{cvtpk(bflo(hh0) + bflo(f0), bfhi(hh0) + bfhi(f0)), cvtpk(bflo(hh1) + bflo(f1), bfhi(hh1) + bfhi(f1))}, mkrs(out, real ? 8 * 576 * 2 : 0), (ob_ * 576 + oc) * 2, 0, 0);
        if (unit == 0 && lane == 0) __builtin_amdgcn_raw_buffer_store_b64(v2u{(unsigned)tag, (unsigned)(tag >> 32)}, rt, WS_EPOCH, 0, AUX_SC1);
        STAMP(15);
      }
    }
  }
  }
  asm volatile(".Lfb_code_end:");
}

extern "C" void kernel_launch(void* const* d_in, const int* in_sizes, int n_in, void* d_out, int out_size, void* d_ws, size_t ws_size, hipStream_t stream) {
  fused_block<<<256, 512, 0, stream>>>((const u16*)d_in[0], (const u16*)d_in[1], (const u16*)d_in[2], (const u16*)d_in[3], (const u16*)d_in[4],
      (const u16*)d_in[6], (const u16*)d_in[7], (const u16*)d_in[8], (u16*)d_out, (unsigned char*)d_ws);
}
```

```cpp
#define PK 1
#include <hip/hip_runtime.h>
#include <hip/hip_bf16.h>

typedef unsigned short u16;
typedef unsigned long long u64;
typedef unsigned v4u __attribute__((ext_vector_type(4)));
typedef unsigned v2u __attribute__((ext_vector_type(2)));
typedef float f32x4 __attribute__((ext_vector_type(4)));
typedef short bf16x8 __attribute__((ext_vector_type(8)));
typedef short s16x4 __attribute__((ext_vector_type(4)));
typedef __bf16 bf16x2_t __attribute__((ext_vector_type(2)));
typedef float f32x2_t __attribute__((ext_vector_type(2)));
#define LAS __attribute__((address_space(3)))
#define GAS __attribute__((address_space(1)))
typedef __amdgpu_buffer_rsrc_t rsrc_t;

#define STAMPS 0
#define DELTA_F 5
#define DELTA_O 4
#define N_EARLY 144
#define DOWN_EARLY 0

#define OPRIV 0
#define OSEG 1024
#define OROW 64
#define LSE_STR 16
#define WS_LSE_OFF (3 << 20)
constexpr int WS_EPOCH = 0;
constexpr int WS_DONE = 98304;
constexpr int WS_LSE = WS_LSE_OFF;
constexpr int WS_H = 16384;
constexpr int WS_QG = 131072;
constexpr int WS_O = WS_QG + 8 * 4 * 4096 * 4;
constexpr int WS_P = WS_O + 16 * 8192;
constexpr int WS_END = WS_LSE_OFF + 256 * 128;
constexpr int WS_DBG = 4 << 20;
constexpr int L_GATE = 0, L_UP = 73728, L_WO = 0, L_RED1 = 131072, L_WQ = 129024;
constexpr int L_XN = 147456, XN_STR = 1168, HN_STR = 1216;
constexpr int L_REC = L_XN, REC_STR = 1536;
constexpr int L_ACT = L_XN + 8 * HN_STR, ACT_STR = 144;
constexpr int L_RED = L_ACT + 8 * ACT_STR;
constexpr int L_Q = L_RED + 1152;
constexpr int L_FLG = L_Q + 512;
constexpr int L_END = L_FLG + 256;
static_assert(L_END <= 163840, "LDS");
constexpr int L_SCR = L_END;
constexpr int L_TOTAL = L_SCR + 2048 + 256;
static_assert(L_TOTAL <= 163840, "LDS");
constexpr int F_QREADY = 0, F_QDONE = 1, F_CHAIN = 2, F_TICK = 3, F_GOT = 4, F_XN = 5, F_FULL = 6, F_TAG = 8, F_S0 = 10, F_FIN = 12, F_END = 14;
#define CODE_PF 3
#define TLB_PF 1
#define EXP 0
#define MAPV 0
#define PACE 0
#define TAU 55
#define T_TARGET 3850
#define LEAD_F 60
#define LEAD_O 0
constexpr u64 TAG_STEP = 0x9E3779ull << 40 | 0x16Bull << 30 | 0x2D5ull << 20 | 0x0F3ull << 10 | 0x1B5ull;
#define TAG_FIELDS(t) v4u{(unsigned)(t) & 0x3ffu, ((unsigned)(t) >> 10) & 0x3ffu, ((unsigned)(t) >> 20) & 0x3ffu, (unsigned)((t) >> 30) & 0x3ffu}
#define WQ_AHEAD 12
#define ROT_ON 1
#define WINDOW 9
#define WIN() asm volatile("s_waitcnt vmcnt(" STR(WINDOW) ")" ::: "memory")
#define HP_DELAY 6
#define RP_DELAY 0
#define DONE_SLEEP 2
#define NHINT 2
#define RH_DELAY 16
#define DE 0
#define DE_DELAY 1
#define PHASE 1
#define XBASE xbase
#define ADAPT 1
#define WINDOW2 15
#define WIN2() do { } while (0)
#define KAUX 2
#define VAUX 2
#define WAUX 2
#define POL_0 ""
#define POL_2 "nt"
#define POL_3 "sc0 nt"
#define POL_18 "sc1 nt"
#define POL_19 "sc0 sc1 nt"
#define POL_1 "sc0"
#define POL_16 "sc1"
#define POL_17 "sc0 sc1"
#define POLCAT2(x) POL_##x
#define POLCAT(x) POLCAT2(x)
#define KPOL POLCAT(KAUX)
#define VPOL POLCAT(VAUX)
#define WPOL POLCAT(WAUX)
#define PPOLL_AUX AUX_SC1
#define QPOLL_AUX AUX_SC1
#define QPOLL_DELAY 50
#define MAXL 8
#define DMA_INFL 12
#define STR2(x) #x
#define STR(x) STR2(x)

constexpr int AUX_NT = 2, AUX_SC1 = 16, AUX_SC1NT = 18;
constexpr unsigned OOB = 0x7FFFFFF0u;
constexpr int SPIN_CAP = 1 << 17, LDS_SPIN_CAP = 1 << 22;
constexpr float LOG2E = 1.4426950408889634f;

#define SB() __builtin_amdgcn_sched_barrier(0)
#define CBAR() asm volatile("" ::: "memory")
#define WG_BARRIER() do { asm volatile("s_waitcnt lgkmcnt(0)\n\ts_barrier" ::: "memory"); } while (0)

__device__ __forceinline__ f32x4 mfma32(v4u a, v4u b, f32x4 c) {
  return __builtin_amdgcn_mfma_f32_16x16x32_bf16(__builtin_bit_cast(bf16x8, a), __builtin_bit_cast(bf16x8, b), c, 0, 0, 0);
}
__device__ __forceinline__ f32x4 mfma16(v2u a, v2u b, f32x4 c) {
  return __builtin_amdgcn_mfma_f32_16x16x16bf16_1k(__builtin_bit_cast(s16x4, a), __builtin_bit_cast(s16x4, b), c, 0, 0, 0);
}
__device__ __forceinline__ unsigned cvtpk(float lo, float hi) {
  f32x2_t v = {lo, hi};
  return __builtin_bit_cast(unsigned, __builtin_convertvector(v, bf16x2_t));
}
__device__ __forceinline__ float rbf(float x) { return __uint_as_float(cvtpk(x, x) & 0xffff0000u); }
__device__ __forceinline__ float bflo(unsigned x) { return __uint_as_float(x << 16); }
__device__ __forceinline__ float bfhi(unsigned x) { return __uint_as_float(x & 0xffff0000u); }
template <int CTRL> __device__ __forceinline__ float dppf(float x) {
  return __builtin_bit_cast(float, __builtin_amdgcn_mov_dpp(__builtin_bit_cast(int, x), CTRL, 0xf, 0xf, true));
}
template <int CTRL> __device__ __forceinline__ unsigned dppu(unsigned x) { return (unsigned)__builtin_amdgcn_mov_dpp((int)x, CTRL, 0xf, 0xf, true); }
constexpr int QP_XOR1 = 0xB1, QP_XOR2 = 0x4E, ROR4 = 0x124, ROR8 = 0x128, HALF_MIRROR = 0x141;
constexpr int QP_0011 = 0x50, QP_2233 = 0xFA, QP_0000 = 0x00, QP_1111 = 0x55, QP_ID = 0xE4;
__device__ __forceinline__ float xrow_max(float x) {
  auto s = __builtin_amdgcn_permlane16_swap(__float_as_uint(x), __float_as_uint(x), false, false);
  x = fmaxf(__uint_as_float(s[0]), __uint_as_float(s[1]));
  auto t = __builtin_amdgcn_permlane32_swap(__float_as_uint(x), __float_as_uint(x), false, false);
  return fmaxf(__uint_as_float(t[0]), __uint_as_float(t[1]));
}
__device__ __forceinline__ float xrow_sum(float x) {
  auto s = __builtin_amdgcn_permlane16_swap(__float_as_uint(x), __float_as_uint(x), false, false);
  x = __uint_as_float(s[0]) + __uint_as_float(s[1]);
  auto t = __builtin_amdgcn_permlane32_swap(__float_as_uint(x), __float_as_uint(x), false, false);
  return __uint_as_float(t[0]) + __uint_as_float(t[1]);
}
__device__ __forceinline__ unsigned xrow_xor(unsigned x) {
  auto s = __builtin_amdgcn_permlane16_swap(x, x, false, false);
  x = s[0] ^ s[1];
  auto t = __builtin_amdgcn_permlane32_swap(x, x, false, false);
  return t[0] ^ t[1];
}
__device__ __forceinline__ float row2_sum(float x) {
  auto s = __builtin_amdgcn_permlane16_swap(__float_as_uint(x), __float_as_uint(x), false, false);
  return __uint_as_float(s[0]) + __uint_as_float(s[1]);
}
__device__ __forceinline__ float wave_sum(float x) {
  x += dppf<QP_XOR1>(x); x += dppf<QP_XOR2>(x); x += dppf<ROR4>(x); x += dppf<ROR8>(x);
  return xrow_sum(x);
}
template <int SRC, int DST> __device__ __forceinline__ unsigned bank_mov(unsigned old, unsigned x) {
  constexpr int sh = ((DST - SRC) & 3) * 4;
  constexpr int ctrl = sh == 0 ? QP_ID : (0x120 + sh);
  return (unsigned)__builtin_amdgcn_update_dpp((int)old, (int)x, ctrl, 0xf, 1 << DST, false);
}
__device__ __forceinline__ rsrc_t mkrs(const void* p, unsigned bytes) { return __builtin_amdgcn_make_buffer_rsrc((void*)p, 0, bytes, 0x00020000); }
__device__ __forceinline__ v2u trread(LAS unsigned char* p) {
  return __builtin_bit_cast(v2u, __builtin_amdgcn_ds_read_tr16_b64_v4i16((LAS s16x4*)p));
}
__device__ __forceinline__ v4u trfrag(LAS unsigned char* p, int rowstride4) {
  const v2u a = trread(p), b = trread(p + rowstride4);
  return v4u{a.x, a.y, b.x, b.y};
}
__device__ __forceinline__ v4u tagged(f32x4 v, v4u t) {
  return v4u{(__float_as_uint(v.x) & ~0x3ffu) | t.x, (__float_as_uint(v.y) & ~0x3ffu) | t.y, (__float_as_uint(v.z) & ~0x3ffu) | t.z, (__float_as_uint(v.w) & ~0x3ffu) | t.w};
}
__device__ __forceinline__ void untag(v4u& v, v4u t, unsigned& bad) { v ^= t; bad |= (v.x | v.y) | (v.z | v.w); }
__device__ __forceinline__ void lds_wait_ge(volatile LAS unsigned* p, unsigned v) {
  for (int s = 0; *p < v && s < LDS_SPIN_CAP; ++s) __builtin_amdgcn_s_sleep(1);
  CBAR();
}
__device__ __forceinline__ void lds_spin_ge(volatile LAS unsigned* p, unsigned v) {
  for (int s = 0; *p < v && s < LDS_SPIN_CAP; ++s) { }
  CBAR();
}
__device__ __forceinline__ void ln_row(const float (&v)[12], bool live, v2u (&out)[3]) {
  const float ref = __uint_as_float(__builtin_amdgcn_readfirstlane(__float_as_uint(v[0])));
  float s1 = 0.f, s2 = 0.f;
#pragma unroll
  for (int i = 0; i < 12; ++i) { const float d = live ? v[i] - ref : 0.f; s1 += d; s2 = fmaf(d, d, s2); }
  s1 = wave_sum(s1); s2 = wave_sum(s2);
  const float md = s1 * (1.f / 576.f);
  const float var = fmaxf(fmaf(-md, md, s2 * (1.f / 576.f)), 0.f);
  const float m = rbf(ref + md);
  const float den = rbf(__builtin_amdgcn_sqrtf(rbf(rbf(var) + 1e-5f)));
  const float inv = __builtin_amdgcn_rcpf(den);
#pragma unroll
  for (int i = 0; i < 3; ++i) {
    const unsigned a = cvtpk(v[4 * i] - m, v[4 * i + 1] - m), c = cvtpk(v[4 * i + 2] - m, v[4 * i + 3] - m);
    out[i] = v2u{cvtpk(bflo(a) * inv, bfhi(a) * inv), cvtpk(bflo(c) * inv, bfhi(c) * inv)};
  }
}

#define STAMP(i) do { } while (0)

__global__ void __launch_bounds__(512, 2) fused_block(const u16* __restrict__ x, const u16* __restrict__ Kc, const u16* __restrict__ Vc, const u16* __restrict__ wq,
    const u16* __restrict__ wo, const u16* __restrict__ wgt, const u16* __restrict__ wup, const u16* __restrict__ wdn, u16* out, unsigned char* ws) {
  __shared__ __attribute__((aligned(16))) unsigned char smem_[L_TOTAL];
  LAS unsigned char* L = (LAS unsigned char*)smem_;
  volatile LAS unsigned* FLG = (volatile LAS unsigned*)(L + L_FLG);
  const int tid = threadIdx.x, lane = tid & 63, w = __builtin_amdgcn_readfirstlane(tid >> 6);
  const unsigned pt0 = (unsigned)__builtin_amdgcn_s_memrealtime();
#define PROBE(k) do { if (PK == (k)) __hip_atomic_store((GAS unsigned*)(ws + (5 << 20) + blockIdx.x * 4), (k) < 2 ? pt0 : (unsigned)__builtin_amdgcn_s_memrealtime(), __ATOMIC_RELAXED, __HIP_MEMORY_SCOPE_AGENT); } while (0)
#define MKDESC(p, bytes) v4u{(unsigned)(u64)(p), (unsigned)((u64)(p) >> 32) & 0xffffu, (unsigned)(bytes), 0x00020000u}
  const unsigned lbase = (unsigned)(size_t)L;
#define DMA_ASMP(pol, desc, ldsa, voff, soff, imm) asm volatile("s_mov_b32 m0, %0\n\ts_nop 0\n\tbuffer_load_dwordx4 %1, %2, %3 offen offset:%4 " pol " lds" \
    :: "s"((ldsa) - (imm)), "v"(voff), "s"(desc), "s"(soff), "i"(imm) : "memory")
#define DMA_ASM(desc, ldsa, voff, soff, imm) DMA_ASMP(WPOL, desc, ldsa, voff, soff, imm)
  const bool live = lane < 48, two = (w == 2 || w == 7);
  const unsigned sw2 = (unsigned)((((lane & 7) ^ (((lane >> 4) & 1) << 1)) * 16) ^ (w << 6));
  v2u ep, xpk[3], xp2[3];
  if (w < 2) {
    {
      u64 pc; unsigned len;
      asm volatile("s_getpc_b64 %0\n.Lfb_code_begin:\n\ts_mov_b32 %1, .Lfb_code_end-.Lfb_code_begin" : "=s"(pc), "=s"(len));
      const v4u dc = MKDESC(pc, len);
      asm volatile("s_mov_b32 m0, %0\n\ts_nop 0\n\tbuffer_load_dword %1, %2, 0 offen lds\n\tbuffer_load_dword %3, %2, 0 offen lds"
        :: "s"(lbase + L_SCR + 2048), "v"(lane * 128 + w * 8192), "s"(dc), "v"(lane * 128 + w * 8192 + 16384) : "memory"); }
    const v4u dq = MKDESC(wq, 576 * 4096 * 2);
    const unsigned vo = (unsigned)((blockIdx.x & 3) * 144 + w * 8 + (lane >> 3)) * 8192u + (blockIdx.x >> 2) * 128 + sw2;
#pragma unroll
    for (int i = 0; i < 9; ++i) DMA_ASM(dq, lbase + L_WQ + w * 1024 + i * 2048, vo, i * 16 * 8192, 0);
    asm volatile("" : "=v"(ep), "=v"(xpk[0]), "=v"(xpk[1]), "=v"(xpk[2]), "=v"(xp2[0]), "=v"(xp2[1]), "=v"(xp2[2]));
  } else {
    const unsigned xo = (unsigned)(8 * (live ? lane : 47)), xo1 = xo + w * 1152, xo2 = xo + (w & 1) * 1152, z = 0;
    asm volatile("global_load_dwordx2 %0, %3, %4\n\tglobal_load_dwordx2 %1, %3, %4 offset:384\n\tglobal_load_dwordx2 %2, %3, %4 offset:768"
      : "=&v"(xpk[0]), "=&v"(xpk[1]), "=&v"(xpk[2]) : "v"(xo1), "s"(x) : "memory");
    asm volatile("global_load_dwordx2 %0, %1, %2 sc1" : "=&v"(ep) : "v"(z), "s"(ws) : "memory");
    if (two) asm volatile("global_load_dwordx2 %0, %3, %4\n\tglobal_load_dwordx2 %1, %3, %4 offset:384\n\tglobal_load_dwordx2 %2, %3, %4 offset:768"
      : "=&v"(xp2[0]), "=&v"(xp2[1]), "=&v"(xp2[2]) : "v"(xo2), "s"(x) : "memory");
    else asm volatile("" : "=v"(xp2[0]), "=v"(xp2[1]), "=v"(xp2[2]));
    if (w == 2) FLG[lane] = 0u;
  }
  WG_BARRIER();
  int wg = blockIdx.x;
  asm volatile("" : "+s"(wg));
  const int uu = MAPV == 0 ? wg : (MAPV == 1 ? (wg & 7) * 32 + (wg >> 3) : (MAPV == 2 ? ((wg * 37 + (wg >> 3) * 0) & 255) ^ ((wg >> 3) & 7) : (MAPV == 3 ? (wg ^ ((wg >> 3) & 7) ^ ((wg >> 6) & 3)) : (int)(__builtin_bitreverse32(wg) >> 24))));
  const int pair = uu >> 1, half = uu & 1, b = pair >> 4, kvh = pair & 15;
  const int rp = (pair & 7) * 16 + (pair >> 3);
  const int hsel = (pair >> 3) & 1;
  const bool holder = half == hsel;
  const bool isF = holder && rp < 72, isO = holder && rp >= 72 && rp < 108;
  const int fu = rp;
  const int sk = (rp - 72) & 3, sn = (rp - 72) >> 2;
  const int cb = wg >> 2, kq = wg & 3;
  const int n = lane & 15, g = lane >> 4, tq_ = (lane & 15) >> 2, tp = lane & 3;
  const int f2 = (tq_ >> 1) | ((g & 1) << 1);
  const rsrc_t rws = mkrs(ws, WS_END);
  const int dl = rp < 72 ? DELTA_F : (rp < 108 ? DELTA_O : 0);
  const int nt0 = 64 + (hsel ? dl : -dl);
  const int NT = half ? 128 - nt0 : nt0;
  const unsigned tsched = (unsigned)__builtin_amdgcn_s_memrealtime() + (unsigned)(T_TARGET - (isF ? LEAD_F : (isO ? LEAD_O : 0)) - NT * TAU);
  const unsigned tbase = ((unsigned)pair << 20) + (unsigned)(half * 64 + 64 - NT) * 8192u, xbase = ((unsigned)pair << 20) + (unsigned)((1 - half) * 64) * 8192u;
  const int voffK = (n >> 1) * 256 + (2 * g + (n & 1)) * 16;
  const int voffV = g * 2048 + n * 16;
  v4u kr[8], vr[8];
#define KOFF(i) (voffK + (((i) >> 1) & 1) * 2048 + ((i) & 1) * 128)
#define LDK(i, so) kr[i] = __builtin_amdgcn_raw_buffer_load_b128(rk, KOFF(i), (so) + ((i) >> 2) * 4096, KAUX)
#define LDV(i, so) vr[i] = __builtin_amdgcn_raw_buffer_load_b128(rv, voffV + (i) * 256, (so), VAUX)
  const rsrc_t rk = mkrs(Kc, 128u << 20), rv = mkrs(Vc, 128u << 20);
#define DMA_PACE() asm volatile("s_waitcnt vmcnt(" STR(DMA_INFL) ")" ::: "memory")
#define LDS_INC(f) do { if (lane == 0) __hip_atomic_fetch_add((LAS unsigned*)(FLG + (f)), 1u, __ATOMIC_RELAXED, __HIP_MEMORY_SCOPE_WORKGROUP); } while (0)
  const rsrc_t rx = mkrs(x, 8 * 576 * 2);
  u64 tag = 0;
  const unsigned guvo = (unsigned)(lane >> 3) * 9216u + fu * 128 + (((lane & 7) ^ ((lane >> 4) & 1)) * 16);
#define GU_DMA(i) do { const int mt_ = (i) >= 72, ri_ = (i) - 72 * mt_; const v4u dg_ = MKDESC(mt_ ? wup : wgt, 576 * 4608 * 2); \
    DMA_ASM(dg_, lbase + (i) * 1024, guvo ^ (unsigned)(((ri_ & 1) << 5) | (mt_ << 6)), ri_ * 8 * 9216, 0); } while (0)
  const int tl0 = isF ? N_EARLY * 1024 : (isO ? 131072 : 0);
  v4u de[8];
  const int dbk = w - 2;
#define DOWN_LOADS(arr, lo, hi) do { const rsrc_t rd = mkrs(wdn, dsz); \
    const unsigned vo_ = (dbk < 4 || n < 8) ? (unsigned)(fu * 64 + 8 * g) * 1152u + dbk * 256 + n * 16 : OOB; \
    _Pragma("unroll") for (int i = (lo); i < (hi); ++i) arr[i] = __builtin_amdgcn_raw_buffer_load_b128(rd, vo_, ((i >> 3) * 32 + (i & 7)) * 1152, WAUX); } while (0)
#define DOWN_TR(arr, o_) do { v4u t_[8]; \
      _Pragma("unroll") for (int jj = 0; jj < 8; ++jj) _Pragma("unroll") for (int mm = 0; mm < 4; ++mm) \
        t_[jj][mm] = __builtin_amdgcn_perm(arr[(o_) + 2 * mm + 1][jj >> 1], arr[(o_) + 2 * mm][jj >> 1], (jj & 1) ? 0x07060302u : 0x05040100u); \
      _Pragma("unroll") for (int jj = 0; jj < 8; ++jj) { arr[(o_) + jj] = t_[jj]; asm volatile("" : "+v"(arr[(o_) + jj])); } } while (0)

  const int nrounds = max((147456 - 4096 - tl0) >> 15, 0);
  if (w < 2) {
    static_assert(DMA_INFL == 12, "the wq signal sits behind the 12th piece");
    const int nw = isF ? N_EARLY : (isO ? 128 : 0);
#define LSTAMP() do { } while (0)
    LSTAMP();
#pragma unroll 1
    for (int run = 0; run < 2; ++run) {
      const int p0 = isF ? 72 * run : 64 * run, p1 = min(nw, isF ? p0 + 72 : p0 + 64);
      if (p1 <= p0) break;
      const v4u dw = isF ? MKDESC(run ? wup : wgt, 576 * 4608 * 2) : MKDESC(wo, 4096 * 576 * 2);
      const unsigned vo = isF ? (guvo ^ (unsigned)((w << 5) | (run << 6))) : (unsigned)(sk * 1024 + (lane >> 3)) * 1152u + sn * 128 + sw2;
      const unsigned sstep = isF ? 16u * 9216u : 16u * 1152u;
      unsigned so = (isF ? 0u : (unsigned)p0 * 8u * 1152u) + (sstep >> 1) * w, la = lbase + (p0 + w) * 1024;
      if (run) lds_wait_ge(FLG + F_QDONE, 4u);
#pragma unroll 1
      for (int i = p0; i < p1; i += 8) {
#pragma unroll
        for (int j = 0; j < 4; ++j) { DMA_ASM(dw, la, vo, so, 0); DMA_PACE(); la += 2048; so += sstep; }
        LSTAMP();
        if (i == 16) LDS_INC(F_XN);
      }
    }
    const v4u dk = MKDESC(Kc, 128u << 20), dv = MKDESC(Vc, 128u << 20);
#pragma unroll 1
    for (int rd = 0; rd < nrounds; ++rd) {
      const int slot = 2 * rd + w;
      if (tl0 + 4096 + (slot + 1) * 16384 > L_WQ) lds_wait_ge(FLG + F_QDONE, 4u);
      const unsigned so = ((PHASE && slot < NT - 64) ? XBASE : tbase) + (unsigned)slot * 8192u, la = lbase + tl0 + 4096 + slot * 16384;
#pragma unroll
      for (int i = 0; i < 8; ++i) { DMA_ASMP(KPOL, dk, la + i * 1024, voffK, so + (i >> 2) * 4096, ((i >> 1) & 1) * 2048 + (i & 1) * 128); DMA_PACE(); if ((i & 3) == 3) LSTAMP(); }
#pragma unroll
      for (int i = 0; i < 8; ++i) { DMA_ASMP(VPOL, dv, la + 8192 + i * 1024, voffV, so, i * 256); DMA_PACE(); if ((i & 3) == 3) LSTAMP();
        if (i == 3 && rd == 0 && nw == 0) LDS_INC(F_XN); }
      FLG[F_FULL + w] = rd;
    }
    SB();
#pragma unroll
    for (int i = 0; i < 8; ++i) { LDK(i, tbase + (2 * nrounds + w) * 8192); SB(); DMA_PACE(); if ((i & 3) == 3) LSTAMP(); }
#pragma unroll
    for (int i = 0; i < 8; ++i) { LDV(i, tbase + (2 * nrounds + w) * 8192); SB(); DMA_PACE(); if ((i & 3) == 3) LSTAMP(); }
    FLG[F_FULL + w] = nrounds;
  } else {
    if (two) asm volatile("s_waitcnt vmcnt(4)" ::: "memory"); else asm volatile("s_waitcnt vmcnt(1)" ::: "memory");
    asm volatile("" : "+v"(xpk[0]), "+v"(xpk[1]), "+v"(xpk[2]));
#define LN_TO_XN(src, row) do { float v[12]; \
      _Pragma("unroll") for (int i = 0; i < 3; ++i) { v[4 * i] = bflo(src[i].x); v[4 * i + 1] = bfhi(src[i].x); v[4 * i + 2] = bflo(src[i].y); v[4 * i + 3] = bfhi(src[i].y); } \
      v2u o3[3]; ln_row(v, live, o3); \
      if (live) { _Pragma("unroll") for (int i = 0; i < 3; ++i) *(LAS v2u*)(L + L_XN + (row) * XN_STR + (192 * i + 4 * lane) * 2) = o3[i]; } } while (0)
    LN_TO_XN(xpk, w);
    SB();
    asm volatile("s_waitcnt vmcnt(0)" : "+v"(ep), "+v"(xp2[0]), "+v"(xp2[1]), "+v"(xp2[2]) :: "memory");
    if (two) LN_TO_XN(xp2, w & 1);
    asm volatile("s_waitcnt lgkmcnt(0)" ::: "memory");
    LDS_INC(F_XN);
    tag = ((u64)(unsigned)__builtin_amdgcn_readfirstlane(ep.y) << 32 | (unsigned)__builtin_amdgcn_readfirstlane(ep.x)) + TAG_STEP;
    const v4u tg = TAG_FIELDS(tag);
    if (w == 7) { FLG[F_TAG] = (unsigned)tag; FLG[F_TAG + 1] = (unsigned)(tag >> 32); }
    const v4u tq4 = tg & 15u;
    if (!two) {
      lds_spin_ge(FLG + F_XN, 8u);
      const int tq = w - 3;
      v4u a[5], bb[5];
#pragma unroll
      for (int ks = 0; ks < 5; ++ks) {
        const int gg = ks < 4 ? g : (g & 1);
        a[ks] = trfrag(L + L_WQ + (32 * ks + 8 * gg + tq_) * 128 + ((tq ^ f2) * 32) + tp * 8, 4 * 128);
        bb[ks] = *(LAS v4u*)(L + L_XN + (n & 7) * XN_STR + (kq * 144 + 32 * ks + 8 * gg) * 2);
      }
      SB();
      if (g >= 2) bb[4] = v4u{0, 0, 0, 0};
      f32x4 d = {0.f, 0.f, 0.f, 0.f};
#pragma unroll
      for (int ks = 0; ks < 5; ++ks) d = mfma32(a[ks], bb[ks], d);
      __builtin_amdgcn_raw_buffer_store_b128((__builtin_bit_cast(v4u, d) & ~15u) | tq4, rws, n < 8 ? WS_QG + ((n * 4 + kq) * 4096 + cb * 64 + tq * 16 + 4 * g) * 4 : OOB, 0, AUX_SC1);
      LDS_INC(F_QDONE);
      STAMP(2);
    }
    if (w == 7) {
      const int qo = WS_QG + (b * 4 * 4096 + kvh * 256 + 4 * lane) * 4;
      v4u a[4];
      if (QPOLL_DELAY) __builtin_amdgcn_s_sleep(QPOLL_DELAY);
#pragma unroll 1
      for (int s = 0; s < SPIN_CAP; ++s) {
#pragma unroll
        for (int i = 0; i < 4; ++i) a[i] = __builtin_amdgcn_raw_buffer_load_b128(rws, qo, i * 16384, QPOLL_AUX);
        unsigned bad = 0;
#pragma unroll
        for (int i = 0; i < 4; ++i) untag(a[i], tq4, bad);
        bad |= dppu<QP_XOR1>(bad); bad |= dppu<QP_XOR2>(bad);
        if ((bad & 15u) == 0u) break;
        __builtin_amdgcn_s_sleep(2);
        CBAR();
      }
#define QV(p) __uint_as_float((p) | 8u)
#define QS(c) ((QV(a[0].c) + QV(a[1].c)) + (QV(a[2].c) + QV(a[3].c)))
      *(LAS v2u*)(L + L_Q + lane * 8) = v2u{cvtpk(QS(x), QS(y)), cvtpk(QS(z), QS(w))};
      asm volatile("s_waitcnt lgkmcnt(0)" ::: "memory");
      FLG[F_QREADY] = 1u;
      PROBE(0); PROBE(1); PROBE(2); PROBE(7);
      STAMP(2);
      {
        unsigned j0 = __builtin_amdgcn_raw_buffer_load_b32(rws, lane < 2 ? WS_O + lane * (1200 << 10) : OOB, 0, AUX_NT);
        unsigned j1 = __builtin_amdgcn_raw_buffer_load_b32(mkrs(out, 8 * 576 * 2), lane == 0 ? 0 : OOB, 0, AUX_NT);
        unsigned j2 = __builtin_amdgcn_raw_buffer_load_b32(mkrs(wdn, 4608 * 576 * 2), (isF && lane < 2) ? (fu * 64 + lane * 63) * 1152 : OOB, 0, AUX_NT);
        asm volatile("" :: "v"(j0), "v"(j1), "v"(j2)); }
    }
#pragma unroll
    for (int i = 0; i < 8; ++i) asm volatile("" : "=v"(kr[i]), "=v"(vr[i]));
  }
  if (w != 7) {
    lds_wait_ge(FLG + F_QREADY, 1u);
    STAMP(4);
    v4u qf[8];
    { const v4u qa0 = *(LAS v4u*)(L + L_Q + (((n >> 1) & 1) * 128 + 16 * g + 8 * (n & 1)) * 2), qa1 = *(LAS v4u*)(L + L_Q + (((n >> 1) & 1) * 128 + 64 + 16 * g + 8 * (n & 1)) * 2);
#pragma unroll
      for (int i = 0; i < 8; ++i) qf[i] = ((n >> 2) == (i >> 1)) ? ((i & 1) ? qa1 : qa0) : v4u{0, 0, 0, 0}; }
    const bool par = lane & 1;
    const unsigned jbmask = par ? 0xffff0000u : 0x0000ffffu;
    float m = -INFINITY, l = 0.f;
    f32x4 o = {0.f, 0.f, 0.f, 0.f};
    v4u pa0 = {0, 0, 0, 0}, pa1 = {0, 0, 0, 0};
#define PV(i, pa, xs_, MORE) { pa.x = bank_mov<((i) >> 1), 0>(pa.x, xs_); pa.y = bank_mov<((i) >> 1), 1>(pa.y, xs_); pa.z = bank_mov<((i) >> 1), 2>(pa.z, xs_); pa.w = bank_mov<((i) >> 1), 3>(pa.w, xs_); \
        o = mfma32(pa, vr[i], o); if (MORE) { WINA(); LDV(i, so); } SB(); }
#define BODY(MORE) { \
      f32x4 d = {0.f, 0.f, 0.f, 0.f}; \
      _Pragma("unroll") for (int i = 0; i < 8; ++i) { d = mfma32(qf[i], kr[i], d); if (MORE) { WINA(); LDK(i, so); } SB(); } \
      const float keep = par ? d.w : d.x, send = par ? d.y : d.z; \
      float s = keep + dppf<QP_XOR1>(send); \
      s = rbf(rbf(s) * 0.08838834764831845f); \
      if (__any(s > m)) { \
        float t = s; t = fmaxf(t, dppf<QP_XOR2>(t)); t = fmaxf(t, dppf<ROR4>(t)); t = fmaxf(t, dppf<ROR8>(t)); t = xrow_max(t); \
        const float mn = fmaxf(m, t); \
        const float al = __builtin_amdgcn_exp2f((m - mn) * LOG2E); \
        m = mn; l *= al; \
        const float a0 = dppf<QP_0000>(al), a1 = dppf<QP_1111>(al); \
        o.x *= a0; o.y *= a0; o.z *= a1; o.w *= a1; \
      } \
      const float p = __builtin_amdgcn_exp2f((s - m) * LOG2E); \
      l += p; \
      const unsigned p2 = cvtpk(p, p); \
      const unsigned x0 = dppu<QP_0011>(p2) & jbmask, x1 = dppu<QP_2233>(p2) & jbmask; \
      PV(0, pa0, x0, MORE) PV(1, pa1, x1, MORE) PV(2, pa0, x0, MORE) PV(3, pa1, x1, MORE) PV(4, pa0, x0, MORE) PV(5, pa1, x1, MORE) PV(6, pa0, x0, MORE) PV(7, pa1, x1, MORE) }
    if (w < 2) {
#pragma unroll 1
      for (int tnext = 2 * nrounds + 2 + w; tnext < NT; tnext += 2) { const unsigned so = tbase + (unsigned)tnext * 8192u;
        const int ahead = (int)((unsigned)__builtin_amdgcn_s_memrealtime() - tsched - (unsigned)(tnext * TAU));
#define WINA() do { WIN2(); asm volatile("s_cmp_ge_i32 %0, 0\n\ts_cbranch_scc1 1f\n\ts_waitcnt vmcnt(" STR(WINDOW) ")\n1:" :: "s"(ahead) : "memory", "scc"); } while (0)
        BODY(1)
      }
    }
#define REC_O(r) (*(LAS f32x4*)(L + L_REC + (r) * REC_STR + lane * 16))
#define REC_ML(r) (*(LAS f32x2_t*)(L + L_REC + (r) * REC_STR + 1024 + lane * 8))
#define MERGE(ro, rml) do { const float mn_ = fmaxf(m, rml.x), ms_ = mn_ == -INFINITY ? 0.f : mn_; \
      const float ea_ = __builtin_amdgcn_exp2f((m - ms_) * LOG2E), eb_ = __builtin_amdgcn_exp2f((rml.x - ms_) * LOG2E); \
      m = mn_; l = __fadd_rn(__fmul_rn(l, ea_), __fmul_rn(rml.y, eb_)); \
      const float a0_ = dppf<QP_0000>(ea_), a1_ = dppf<QP_1111>(ea_), b0_ = dppf<QP_0000>(eb_), b1_ = dppf<QP_1111>(eb_); \
      o.x = __fadd_rn(__fmul_rn(o.x, a0_), __fmul_rn(ro.x, b0_)); o.y = __fadd_rn(__fmul_rn(o.y, a0_), __fmul_rn(ro.y, b0_)); \
      o.z = __fadd_rn(__fmul_rn(o.z, a1_), __fmul_rn(ro.z, b1_)); o.w = __fadd_rn(__fmul_rn(o.w, a1_), __fmul_rn(ro.w, b1_)); } while (0)
    if (w == 0 && nrounds > 0) {
      lds_wait_ge(FLG + F_CHAIN, 5u);
      const f32x4 ro = REC_O(2); const f32x2_t rml = REC_ML(2); MERGE(ro, rml);
    }
    const u64 tagp = (u64)(unsigned)__builtin_amdgcn_readfirstlane(FLG[F_TAG + 1]) << 32 | (unsigned)__builtin_amdgcn_readfirstlane(FLG[F_TAG]);
#pragma unroll 1
    for (int t = w < 2 ? -1 : w - 2; t < 2 * nrounds; t += 5) {
      if (t >= 0) {
        lds_wait_ge(FLG + F_FULL + (t & 1), (unsigned)((t >> 1) + 1));
#pragma unroll
        for (int i = 0; i < 8; ++i) { kr[i] = *(LAS v4u*)(L + tl0 + 4096 + t * 16384 + i * 1024 + lane * 16); vr[i] = *(LAS v4u*)(L + tl0 + 4096 + t * 16384 + 8192 + i * 1024 + lane * 16); }
      }
      const unsigned so = 0; const int ahead = 0; BODY(0)
      if (t < 0) break;
    }
    STAMP(5);
    bool pub = true;
    if (w >= 2) {
      if (w == 2) lds_wait_ge(FLG + F_QDONE, 4u);
      else lds_wait_ge(FLG + F_CHAIN, (unsigned)(w - 2));
    } else {
      if (FLG[F_TICK] == 0u) {
        REC_O(w) = o; REC_ML(w) = f32x2_t{m, l};
        asm volatile("s_waitcnt lgkmcnt(0)" ::: "memory");
        unsigned tk = 0;
        if (lane == 0) tk = __hip_atomic_fetch_add((LAS unsigned*)(FLG + F_TICK), 1u, __ATOMIC_RELAXED, __HIP_MEMORY_SCOPE_WORKGROUP);
        pub = __builtin_amdgcn_readfirstlane(tk) != 0u;
      }
      CBAR();
    }
    if (pub) {
      if (w != 2) { const int r = w < 2 ? 1 - w : 2; const f32x4 ro = REC_O(r); const f32x2_t rml = REC_ML(r); MERGE(ro, rml); }
      if (w >= 2) { REC_O(2) = o; REC_ML(2) = f32x2_t{m, l}; asm volatile("s_waitcnt lgkmcnt(0)" ::: "memory"); FLG[F_CHAIN] = w - 1; }
      const rsrc_t rp_ = mkrs(ws, w < 2 ? WS_END : 0);
      l += dppf<QP_XOR2>(l); l += dppf<ROR4>(l); l += dppf<ROR8>(l); l = xrow_sum(l);
      const float inv = __builtin_amdgcn_rcpf(l), i0 = dppf<QP_0000>(inv), i1 = dppf<QP_1111>(inv);
      const int slot = kvh * 16 + b * 2 + half;
      const v2u pk = {cvtpk(o.x * i0, o.y * i0), cvtpk(o.z * i1, o.w * i1)};
      __builtin_amdgcn_raw_buffer_store_b64(pk, rp_, WS_O + kvh * 8192 + ((n >> 2) * 2 + (g >> 1)) * OSEG + (b * 2 + half) * OROW + (n & 3) * 16 + (g & 1) * 8, 0, AUX_SC1);
      unsigned ck = pk.x ^ pk.y;
      ck ^= dppu<QP_XOR1>(ck); ck ^= dppu<QP_XOR2>(ck); ck ^= dppu<HALF_MIRROR>(ck); ck = xrow_xor(ck);
      const unsigned lse0 = __float_as_uint(fmaf(m, LOG2E, __builtin_amdgcn_logf(l))), lse1 = dppu<QP_1111>(lse0);
      const unsigned salt = lse0 ^ __builtin_rotateleft32(lse1, 13) ^ (unsigned)(tagp >> 32);
      __builtin_amdgcn_raw_buffer_store_b128(v4u{lse0, lse1, ck ^ salt, dppu<ROR8>(ck) ^ salt}, rp_, lane == 0 ? WS_LSE + slot * LSE_STR : OOB, 0, AUX_SC1);
      if (w < 2) { PROBE(3); PROBE(5); }
    }
  }
  if (DE > 0) {
    if (isF && w >= 2 && w < 7) { const unsigned dsz = 4608 * 576 * 2; for (int z = 0; z < DE_DELAY * (w - 1); ++z) __builtin_amdgcn_s_sleep(127); DOWN_LOADS(de, 0, DE);
      if (DE == 8) DOWN_TR(de, 0);
#pragma unroll
      for (int i = 0; i < DE; ++i) asm volatile("" : "+v"(de[i])); }
    else {
#pragma unroll
      for (int i = 0; i < DE; ++i) asm volatile("" : "=v"(de[i])); }
  }
#pragma unroll 1
  for (int real = (w == 7) ? 0 : 1; real < 2; ++real) {
  const rsrc_t rt = mkrs(ws, real ? WS_END : 0);
  int lane_ = tid & 63;
  asm volatile("" : "+v"(lane_));
  const int lane = lane_, n = lane & 15, g = lane >> 4, tq_ = (lane & 15) >> 2, tp = lane & 3, f2 = (tq_ >> 1) | ((g & 1) << 1);
  const bool live = lane < 48;
  if (real) WG_BARRIER();
  STAMP(7);
  tag = real ? ((u64)(unsigned)__builtin_amdgcn_readfirstlane(FLG[F_TAG + 1]) << 32 | (unsigned)__builtin_amdgcn_readfirstlane(FLG[F_TAG])) : 0ull;
  const v4u tg = TAG_FIELDS(tag);
  const unsigned thint = (unsigned)(tag >> 32);
  if (isO) {
    v4u af[16];
#pragma unroll
    for (int i = 0; i < 16; ++i) af[i] = trfrag(L + L_WO + ((w >> 1) * 256 + (i >> 3) * 128 + (w & 1) * 64 + ((i >> 2) & 1) * 32 + 8 * g + tq_) * 128 + (((i & 3) ^ f2) * 32) + tp * 8, 4 * 128);
    const int kv_ = sk * 4 + (w >> 1);
    v4u of[4], ls;
#pragma unroll 1
    for (int s = 0; s < SPIN_CAP; ++s) {
      ls = __builtin_amdgcn_raw_buffer_load_b128(rt, WS_LSE + (kv_ * 16 + n) * LSE_STR, 0, AUX_SC1);
#pragma unroll
      for (int i = 0; i < 4; ++i) of[i] = __builtin_amdgcn_raw_buffer_load_b128(rt, WS_O + kv_ * 8192 + (w & 1) * 4 * OSEG + n * OROW + g * 16 + i * OSEG, 0, AUX_SC1);
      const v4u xx = (of[0] ^ of[1]) ^ (of[2] ^ of[3]);
      const unsigned ck = xrow_xor((xx.x ^ xx.y) ^ (xx.z ^ xx.w)) ^ ls.x ^ __builtin_rotateleft32(ls.y, 13) ^ thint ^ ((w & 1) ? ls.w : ls.z);
      if (ck == 0u) break;
      __builtin_amdgcn_s_sleep(1);
      CBAR();
    }
    { unsigned tk = 0;
      if (lane == 0 && real) tk = __hip_atomic_fetch_add((LAS unsigned*)(FLG + F_GOT), 1u, __ATOMIC_RELAXED, __HIP_MEMORY_SCOPE_WORKGROUP);
      if (sn == 0 && (__builtin_amdgcn_readfirstlane(tk) == 7u || !real)) {
#pragma unroll
        for (int i = 0; i < NHINT; ++i) __builtin_amdgcn_raw_buffer_store_b32(thint, rt, WS_DONE + lane * 128 + sk * 4, i * 8192, AUX_SC1); } }
    STAMP(9);
#define U2F __uint_as_float
    f32x4 d[8];
#pragma unroll
    for (int t = 0; t < 8; ++t) d[t] = f32x4{0.f, 0.f, 0.f, 0.f};
#pragma unroll
    for (int ks = 0; ks < 2; ++ks) {
      const v4u b0 = {of[2 * ks].x, of[2 * ks].z, of[2 * ks + 1].x, of[2 * ks + 1].z}, b1 = {of[2 * ks].y, of[2 * ks].w, of[2 * ks + 1].y, of[2 * ks + 1].w};
#pragma unroll
      for (int t = 0; t < 4; ++t) { d[t] = mfma32(af[ks * 4 + t], b0, d[t]); d[4 + t] = mfma32(af[8 + ks * 4 + t], b1, d[4 + t]); }
    }
    const float e0 = U2F(ls.x), e1 = U2F(ls.y);
    const float w0 = __builtin_amdgcn_rcpf(1.f + __builtin_amdgcn_exp2f(dppf<QP_XOR1>(e0) - e0)), w1 = __builtin_amdgcn_rcpf(1.f + __builtin_amdgcn_exp2f(dppf<QP_XOR1>(e1) - e1));
    {
      const bool od = n & 1;
      f32x4 r[4];
#pragma unroll
      for (int t = 0; t < 4; ++t) r[t] = w0 * d[t] + w1 * d[4 + t];
      f32x4 k0 = od ? r[2] : r[0], k1 = od ? r[3] : r[1], s0 = od ? r[0] : r[2], s1 = od ? r[1] : r[3];
      k0.x += dppf<QP_XOR1>(s0.x); k0.y += dppf<QP_XOR1>(s0.y); k0.z += dppf<QP_XOR1>(s0.z); k0.w += dppf<QP_XOR1>(s0.w);
      k1.x += dppf<QP_XOR1>(s1.x); k1.y += dppf<QP_XOR1>(s1.y); k1.z += dppf<QP_XOR1>(s1.z); k1.w += dppf<QP_XOR1>(s1.w);
      LAS unsigned char* dst = L + (real ? L_RED1 + w * 2048 : L_SCR) + (n >> 1) * 256 + (od ? 128 : 0) + g * 16;
      *(LAS f32x4*)dst = k0; *(LAS f32x4*)(dst + 64) = k1; }
    if (real) WG_BARRIER();
    {
      f32x4 t = *(LAS f32x4*)(L + L_RED1 + g * 4096 + w * 256 + n * 16) + *(LAS f32x4*)(L + L_RED1 + g * 4096 + 2048 + w * 256 + n * 16);
      t.x = xrow_sum(t.x); t.y = xrow_sum(t.y); t.z = xrow_sum(t.z); t.w = xrow_sum(t.w);
      __builtin_amdgcn_raw_buffer_store_b128(tagged(t, tg), rt, lane < 16 ? WS_H + ((w * 4 + sk) * 576 + sn * 64 + n * 4) * 4 : OOB, 0, AUX_SC1);
      STAMP(10);
    }
  } else if (isF) {
    v4u df[16];
#pragma unroll
    for (int i = 0; i < DE; ++i) df[i] = de[i];
    const bool dwave = (w >= 2 && w < 7) || !real;
    v2u xpk[3];
#pragma unroll
    for (int i = 0; i < 3; ++i) xpk[i] = __builtin_amdgcn_raw_buffer_load_b64(rx, 8 * (live ? lane : 47) + 384 * i, w * 1152, 0);
    SB();
    if (N_EARLY < 144 && real) {
#pragma unroll 1
      for (int i = N_EARLY + w; i < 144; i += 8) GU_DMA(i);
    }
    static_assert(N_EARLY == 144, "");
    if (dwave) { const unsigned dsz = real ? 4608 * 576 * 2 : 0; DOWN_LOADS(df, DE, 16); SB(); }
    else {
#pragma unroll
      for (int i = DE; i < 16; ++i) asm volatile("" : "=v"(df[i]));
    }
    if (N_EARLY < 144 && real) WG_BARRIER();
    SB();
    v4u gf[18];
    { LAS unsigned char* ga = L + ((tp & 1) ? L_UP : L_GATE) + (8 * g + tq_) * 128 + ((w ^ (f2 | ((tp & 1) << 2))) * 16) + (tp >> 1) * 8;
#pragma unroll
      for (int ks = 0; ks < 18; ++ks) gf[ks] = trfrag(ga + ks * 32 * 128, 4 * 128); }
#pragma unroll
    for (int ks = 0; ks < 18; ++ks) asm volatile("" : "+v"(gf[ks]));
    if (dwave) { if (DE != 8) DOWN_TR(df, 0); DOWN_TR(df, 8); }
    asm volatile("" : "+v"(xpk[0]), "+v"(xpk[1]), "+v"(xpk[2]));
    STAMP(6);
#define HINT_WAIT(idx) do { _Pragma("unroll 1") for (int s = 0; s < SPIN_CAP; ++s) { \
        const unsigned f0 = __builtin_amdgcn_raw_buffer_load_b32(rt, WS_DONE + (idx) * 128 + (lane & 3) * 4, 0, AUX_SC1); \
        if (f0 == thint) break; \
        __builtin_amdgcn_s_sleep(DONE_SLEEP); CBAR(); } } while (0)
    if (w == 7) { HINT_WAIT(fu); if (HP_DELAY) __builtin_amdgcn_s_sleep(HP_DELAY); }
    if (real) WG_BARRIER();
    int hoff[3];
#pragma unroll
    for (int i = 0; i < 3; ++i) { const int c = 24 * i + (lane >> 1); hoff[i] = (c & 3) * 288 + (c >> 2) * 16 + (lane & 1) * 8; asm volatile("" : "+v"(hoff[i])); }
    v4u hp[12];
#pragma unroll
    for (int i = 0; i < 12; ++i) asm volatile("" : "=v"(hp[i]));
    STAMP(3);
    unsigned pend = 15u;
#pragma unroll 1
    for (int s = 0; s < SPIN_CAP; ++s) {
#pragma unroll
      for (int q = 0; q < 4; ++q) if ((pend >> q) & 1u) {
#pragma unroll
        for (int i = 3 * q; i < 3 * q + 3; ++i) hp[i] = __builtin_amdgcn_raw_buffer_load_b128(rt, WS_H + (w * 4 * 576 + 4 * (live ? lane : 47)) * 4, i * 768, AUX_SC1); }
      SB();
#pragma unroll
      for (int q = 0; q < 4; ++q) if ((pend >> q) & 1u) {
        unsigned bad = 0;
#pragma unroll
        for (int i = 3 * q; i < 3 * q + 3; ++i) untag(hp[i], tg, bad);
        if (!__any((bad & 0x3ffu) != 0u)) pend &= ~(1u << q); }
      if (pend == 0u) break;
      CBAR();
    }
    STAMP(9);
    { float v[12];
#pragma unroll
      for (int i = 0; i < 3; ++i) {
        const f32x4 a = (__builtin_bit_cast(f32x4, hp[i]) + __builtin_bit_cast(f32x4, hp[3 + i])) + (__builtin_bit_cast(f32x4, hp[6 + i]) + __builtin_bit_cast(f32x4, hp[9 + i]));
        const unsigned r0 = cvtpk(a.x, a.y), r1 = cvtpk(a.z, a.w);
        const unsigned h0 = cvtpk(bflo(xpk[i].x) + bflo(r0), bfhi(xpk[i].x) + bfhi(r0)), h1 = cvtpk(bflo(xpk[i].y) + bflo(r1), bfhi(xpk[i].y) + bfhi(r1));
        v[4 * i] = bflo(h0); v[4 * i + 1] = bfhi(h0); v[4 * i + 2] = bflo(h1); v[4 * i + 3] = bfhi(h1); }
      v2u o3[3]; ln_row(v, live, o3);
      if (live) {
#pragma unroll
        for (int i = 0; i < 3; ++i) *(LAS v2u*)(L + (real ? L_XN + w * HN_STR : L_SCR) + hoff[i]) = o3[i]; } }
    if (real) WG_BARRIER();
    STAMP(11);
    {
      v4u hb[9];
#pragma unroll
      for (int ks = 0; ks < 9; ++ks) hb[ks] = *(LAS v4u*)(L + L_XN + (n & 7) * HN_STR + g * 288 + (n >> 3) * 144 + ks * 16);
      SB();
      f32x4 d = {0.f, 0.f, 0.f, 0.f}, d2 = {0.f, 0.f, 0.f, 0.f};
#pragma unroll
      for (int ks = 0; ks < 9; ++ks) { d = mfma32(gf[ks], hb[ks], d); d2 = mfma32(gf[9 + ks], hb[ks], d2); }
      d.x += dppf<ROR8>(d2.x); d.y += dppf<ROR8>(d2.y); d.z += dppf<ROR8>(d2.z); d.w += dppf<ROR8>(d2.w);
      auto s0 = __builtin_amdgcn_permlane16_swap(__float_as_uint(d.x), __float_as_uint(d.z), false, false);
      auto s1 = __builtin_amdgcn_permlane16_swap(__float_as_uint(d.y), __float_as_uint(d.w), false, false);
      float av[2];
#pragma unroll
      for (int e = 0; e < 2; ++e) {
        const float gg = rbf(__uint_as_float(e ? s1[0] : s0[0])), uu = rbf(__uint_as_float(e ? s1[1] : s0[1]));
        const float z2 = gg * fmaf(gg * gg, 0.044715f, 1.f) * (2.f * 0.7978845608028654f * LOG2E);
        const float ge = rbf(gg * __builtin_amdgcn_rcpf(1.f + __builtin_amdgcn_exp2f(-z2)));
        av[e] = ge * uu; }
      unsigned pk = cvtpk(av[0], av[1]);
      asm volatile("" : "+v"(pk));
      if (n < 8) *(LAS unsigned*)(L + L_ACT + n * ACT_STR + (8 * w + 2 * g) * 2) = pk; }
    if (real) WG_BARRIER();
    STAMP(12);
    if (dwave) {
      const v4u b0 = *(LAS v4u*)(L + L_ACT + (n & 7) * ACT_STR + g * 16), b1 = *(LAS v4u*)(L + L_ACT + (n & 7) * ACT_STR + 64 + g * 16);
      f32x4 d[8];
#pragma unroll
      for (int jj = 0; jj < 8; ++jj) d[jj] = mfma32(df[jj], b0, f32x4{0.f, 0.f, 0.f, 0.f});
#pragma unroll
      for (int jj = 0; jj < 8; ++jj) d[jj] = mfma32(df[8 + jj], b1, d[jj]);
      const bool hq = n >= 8;
      const unsigned po = (dbk < 4 || g < 2) ? WS_P + ((32 * dbk + 8 * g + (n >> 3)) * 72 + fu) * 128 + (n & 7) * 16 : OOB;
#pragma unroll
      for (int e = 0; e < 4; ++e)
        __builtin_amdgcn_raw_buffer_store_b128(tagged(f32x4{hq ? d[4][e] : d[0][e], hq ? d[5][e] : d[1][e], hq ? d[6][e] : d[2][e], hq ? d[7][e] : d[3][e]}, tg), rt, po, 2 * e * 72 * 128, AUX_SC1);
    }
  } else {
    const int unit = rp < 108 ? rp : 108 + (rp - 108) * 2 + half;
    if (unit < 144) {
      const int ob_ = lane & 7, oc = unit * 4;
      v2u xo = {0, 0};
      f32x4 a = {0.f, 0.f, 0.f, 0.f};
      if (w == 7) {
        xo = *(const GAS v2u*)(x + ob_ * 576 + oc);
        v4u hv;
        if (NHINT == 4) { HINT_WAIT(72 + unit); __builtin_amdgcn_s_sleep(RH_DELAY); }
#pragma unroll 1
        for (int s = 0; s < SPIN_CAP; ++s) {
          hv = __builtin_amdgcn_raw_buffer_load_b128(rt, WS_H + ((ob_ * 4 + ((lane >> 3) & 3)) * 576 + oc) * 4, 0, AUX_SC1);
          unsigned bad = 0; untag(hv, tg, bad);
          if ((bad & 0x3ffu) == 0u) break;
          __builtin_amdgcn_s_sleep(NHINT == 4 ? 4 : 16);
          CBAR();
        }
        a = __builtin_bit_cast(f32x4, hv);
        a.x += dppf<ROR8>(a.x); a.y += dppf<ROR8>(a.y); a.z += dppf<ROR8>(a.z); a.w += dppf<ROR8>(a.w);
        a.x = row2_sum(a.x); a.y = row2_sum(a.y); a.z = row2_sum(a.z); a.w = row2_sum(a.w);
        if (RP_DELAY) __builtin_amdgcn_s_sleep(RP_DELAY);
      }
      if (real) WG_BARRIER();
      STAMP(9);
      v4u p0, p1;
#pragma unroll 1
      for (int s = 0; s < SPIN_CAP; ++s) {
        p0 = __builtin_amdgcn_raw_buffer_load_b128(rt, WS_P + unit * 9216 + w * 1024 + lane * 16, 0, PPOLL_AUX);
        p1 = __builtin_amdgcn_raw_buffer_load_b128(rt, WS_P + unit * 9216 + 8192 + w * 128 + (lane & 7) * 16, 0, PPOLL_AUX);
        unsigned bad = 0; untag(p0, tg, bad); untag(p1, tg, bad);
        if ((bad & 0x3ffu) == 0u) break;
        __builtin_amdgcn_s_sleep(1);
        CBAR();
      }
      STAMP(11);
      f32x4 t = __builtin_bit_cast(f32x4, p0);
      t.x += dppf<ROR8>(t.x); t.y += dppf<ROR8>(t.y); t.z += dppf<ROR8>(t.z); t.w += dppf<ROR8>(t.w);
      t.x = xrow_sum(t.x); t.y = xrow_sum(t.y); t.z = xrow_sum(t.z); t.w = xrow_sum(t.w);
      if (lane < 8) *(LAS f32x4*)(L + L_RED + w * 128 + lane * 16) = t + __builtin_bit_cast(f32x4, p1);
      if (real) WG_BARRIER();
      if (w == 7 && lane < 8) {
        f32x4 f = *(LAS f32x4*)(L + L_RED + lane * 16);
#pragma unroll
        for (int r = 1; r < 8; ++r) f += *(LAS f32x4*)(L + L_RED + r * 128 + lane * 16);
        const unsigned r0 = cvtpk(a.x, a.y), r1 = cvtpk(a.z, a.w);
        const unsigned hh0 = cvtpk(bflo(xo.x) + bflo(r0), bfhi(xo.x) + bfhi(r0)), hh1 = cvtpk(bflo(xo.y) + bflo(r1), bfhi(xo.y) + bfhi(r1));
        const unsigned f0 = cvtpk(f.x, f.y), f1 = cvtpk(f.z, f.w);
        __builtin_amdgcn_raw_buffer_store_b64(v2u{cvtpk(bflo(hh0) + bflo(f0), bfhi(hh0) + bfhi(f0)), cvtpk(bflo(hh1) + bflo(f1), bfhi(hh1) + bfhi(f1))}, mkrs(out, real ? 8 * 576 * 2 : 0), (ob_ * 576 + oc) * 2, 0, 0);
        if (unit == 0 && lane == 0) __builtin_amdgcn_raw_buffer_store_b64(v2u{(unsigned)tag, (unsigned)(tag >> 32)}, rt, WS_EPOCH, 0, AUX_SC1);
        STAMP(15);
      }
      if (real && unit == 0 && w == 7) {
        const unsigned now = (unsigned)__builtin_amdgcn_s_memrealtime();
        int best = (PK == 0 || PK == 5 || PK == 7) ? 0x7fffffff : -0x7fffffff;
        for (int i = 0; i < 4; ++i) { const int v = (int)(__hip_atomic_load((GAS unsigned*)(ws + (5 << 20) + (i * 64 + lane) * 4), __ATOMIC_RELAXED, __HIP_MEMORY_SCOPE_AGENT) - now);
          best = (PK == 0 || PK == 5 || PK == 7) ? min(best, v) : max(best, v); }
        for (int o = 32; o > 0; o >>= 1) { const int v = __shfl_xor(best, o); best = (PK == 0 || PK == 5 || PK == 7) ? min(best, v) : max(best, v); }
        if (PK == 6) best = 0;
        while ((int)((unsigned)__builtin_amdgcn_s_memrealtime() - now) < best + 8000) __builtin_amdgcn_s_sleep(2);
      }
    }
  }
  }
  asm volatile(".Lfb_code_end:");
}

extern "C" void kernel_launch(void* const* d_in, const int* in_sizes, int n_in, void* d_out, int out_size, void* d_ws, size_t ws_size, hipStream_t stream) {
  fused_block<<<256, 512, 0, stream>>>((const u16*)d_in[0], (const u16*)d_in[1], (const u16*)d_in[2], (const u16*)d_in[3], (const u16*)d_in[4],
      (const u16*)d_in[6], (const u16*)d_in[7], (const u16*)d_in[8], (u16*)d_out, (unsigned char*)d_ws);
}
```

```cpp
#define PK 0
#include <hip/hip_runtime.h>
#include <hip/hip_bf16.h>

typedef unsigned short u16;
typedef unsigned long long u64;
typedef unsigned v4u __attribute__((ext_vector_type(4)));
typedef unsigned v2u __attribute__((ext_vector_type(2)));
typedef float f32x4 __attribute__((ext_vector_type(4)));
typedef short bf16x8 __attribute__((ext_vector_type(8)));
typedef short s16x4 __attribute__((ext_vector_type(4)));
typedef __bf16 bf16x2_t __attribute__((ext_vector_type(2)));
typedef float f32x2_t __attribute__((ext_vector_type(2)));
#define LAS __attribute__((address_space(3)))
#define GAS __attribute__((address_space(1)))
typedef __amdgpu_buffer_rsrc_t rsrc_t;

#define STAMPS 0
#define DELTA_F 5
#define DELTA_O 4
#define N_EARLY 144
#define DOWN_EARLY 0

#define OPRIV 0
#define OSEG 1024
#define OROW 64
#define LSE_STR 16
#define WS_LSE_OFF (3 << 20)
constexpr int WS_EPOCH = 0;
constexpr int WS_DONE = 98304;
constexpr int WS_LSE = WS_LSE_OFF;
constexpr int WS_H = 16384;
constexpr int WS_QG = 131072;
constexpr int WS_O = WS_QG + 8 * 4 * 4096 * 4;
constexpr int WS_P = WS_O + 16 * 8192;
constexpr int WS_END = WS_LSE_OFF + 256 * 128;
constexpr int WS_DBG = 4 << 20;
constexpr int L_GATE = 0, L_UP = 73728, L_WO = 0, L_RED1 = 131072, L_WQ = 129024;
constexpr int L_XN = 147456, XN_STR = 1168, HN_STR = 1216;
constexpr int L_REC = L_XN, REC_STR = 1536;
constexpr int L_ACT = L_XN + 8 * HN_STR, ACT_STR = 144;
constexpr int L_RED = L_ACT + 8 * ACT_STR;
constexpr int L_Q = L_RED + 1152;
constexpr int L_FLG = L_Q + 512;
constexpr int L_END = L_FLG + 256;
static_assert(L_END <= 163840, "LDS");
constexpr int L_SCR = L_END;
constexpr int L_TOTAL = L_SCR + 2048 + 256;
static_assert(L_TOTAL <= 163840, "LDS");
constexpr int F_QREADY = 0, F_QDONE = 1, F_CHAIN = 2, F_TICK = 3, F_GOT = 4, F_XN = 5, F_FULL = 6, F_TAG = 8, F_S0 = 10, F_FIN = 12, F_END = 14;
#define CODE_PF 3
#define TLB_PF 1
#define EXP 0
#define MAPV 0
#define PACE 0
#define TAU 55
#define T_TARGET 3850
#define LEAD_F 60
#define LEAD_O 0
constexpr u64 TAG_STEP = 0x9E3779ull << 40 | 0x16Bull << 30 | 0x2D5ull << 20 | 0x0F3ull << 10 | 0x1B5ull;
#define TAG_FIELDS(t) v4u{(unsigned)(t) & 0x3ffu, ((unsigned)(t) >> 10) & 0x3ffu, ((unsigned)(t) >> 20) & 0x3ffu, (unsigned)((t) >> 30) & 0x3ffu}
#define WQ_AHEAD 12
#define ROT_ON 1
#define WINDOW 9
#define WIN() asm volatile("s_waitcnt vmcnt(" STR(WINDOW) ")" ::: "memory")
#define HP_DELAY 6
#define RP_DELAY 0
#define DONE_SLEEP 2
#define NHINT 2
#define RH_DELAY 16
#define DE 0
#define DE_DELAY 1
#define PHASE 1
#define XBASE xbase
#define ADAPT 1
#define WINDOW2 15
#define WIN2() do { } while (0)
#define KAUX 2
#define VAUX 2
#define WAUX 2
#define POL_0 ""
#define POL_2 "nt"
#define POL_3 "sc0 nt"
#define POL_18 "sc1 nt"
#define POL_19 "sc0 sc1 nt"
#define POL_1 "sc0"
#define POL_16 "sc1"
#define POL_17 "sc0 sc1"
#define POLCAT2(x) POL_##x
#define POLCAT(x) POLCAT2(x)
#define KPOL POLCAT(KAUX)
#define VPOL POLCAT(VAUX)
#define WPOL POLCAT(WAUX)
#define PPOLL_AUX AUX_SC1
#define QPOLL_AUX AUX_SC1
#define QPOLL_DELAY 50
#define MAXL 8
#define DMA_INFL 12
#define STR2(x) #x
#define STR(x) STR2(x)

constexpr int AUX_NT = 2, AUX_SC1 = 16, AUX_SC1NT = 18;
constexpr unsigned OOB = 0x7FFFFFF0u;
constexpr int SPIN_CAP = 1 << 17, LDS_SPIN_CAP = 1 << 22;
constexpr float LOG2E = 1.4426950408889634f;

#define SB() __builtin_amdgcn_sched_barrier(0)
#define CBAR() asm volatile("" ::: "memory")
#define WG_BARRIER() do { asm volatile("s_waitcnt lgkmcnt(0)\n\ts_barrier" ::: "memory"); } while (0)

__device__ __forceinline__ f32x4 mfma32(v4u a, v4u b, f32x4 c) {
  return __builtin_amdgcn_mfma_f32_16x16x32_bf16(__builtin_bit_cast(bf16x8, a), __builtin_bit_cast(bf16x8, b), c, 0, 0, 0);
}
__device__ __forceinline__ f32x4 mfma16(v2u a, v2u b, f32x4 c) {
  return __builtin_amdgcn_mfma_f32_16x16x16bf16_1k(__builtin_bit_cast(s16x4, a), __builtin_bit_cast(s16x4, b), c, 0, 0, 0);
}
__device__ __forceinline__ unsigned cvtpk(float lo, float hi) {
  f32x2_t v = {lo, hi};
  return __builtin_bit_cast(unsigned, __builtin_convertvector(v, bf16x2_t));
}
__device__ __forceinline__ float rbf(float x) { return __uint_as_float(cvtpk(x, x) & 0xffff0000u); }
__device__ __forceinline__ float bflo(unsigned x) { return __uint_as_float(x << 16); }
__device__ __forceinline__ float bfhi(unsigned x) { return __uint_as_float(x & 0xffff0000u); }
template <int CTRL> __device__ __forceinline__ float dppf(float x) {
  return __builtin_bit_cast(float, __builtin_amdgcn_mov_dpp(__builtin_bit_cast(int, x), CTRL, 0xf, 0xf, true));
}
template <int CTRL> __device__ __forceinline__ unsigned dppu(unsigned x) { return (unsigned)__builtin_amdgcn_mov_dpp((int)x, CTRL, 0xf, 0xf, true); }
constexpr int QP_XOR1 = 0xB1, QP_XOR2 = 0x4E, ROR4 = 0x124, ROR8 = 0x128, HALF_MIRROR = 0x141;
constexpr int QP_0011 = 0x50, QP_2233 = 0xFA, QP_0000 = 0x00, QP_1111 = 0x55, QP_ID = 0xE4;
__device__ __forceinline__ float xrow_max(float x) {
  auto s = __builtin_amdgcn_permlane16_swap(__float_as_uint(x), __float_as_uint(x), false, false);
  x = fmaxf(__uint_as_float(s[0]), __uint_as_float(s[1]));
  auto t = __builtin_amdgcn_permlane32_swap(__float_as_uint(x), __float_as_uint(x), false, false);
  return fmaxf(__uint_as_float(t[0]), __uint_as_float(t[1]));
}
__device__ __forceinline__ float xrow_sum(float x) {
  auto s = __builtin_amdgcn_permlane16_swap(__float_as_uint(x), __float_as_uint(x), false, false);
  x = __uint_as_float(s[0]) + __uint_as_float(s[1]);
  auto t = __builtin_amdgcn_permlane32_swap(__float_as_uint(x), __float_as_uint(x), false, false);
  return __uint_as_float(t[0]) + __uint_as_float(t[1]);
}
__device__ __forceinline__ unsigned xrow_xor(unsigned x) {
  auto s = __builtin_amdgcn_permlane16_swap(x, x, false, false);
  x = s[0] ^ s[1];
  auto t = __builtin_amdgcn_permlane32_swap(x, x, false, false);
  return t[0] ^ t[1];
}
__device__ __forceinline__ float row2_sum(float x) {
  auto s = __builtin_amdgcn_permlane16_swap(__float_as_uint(x), __float_as_uint(x), false, false);
  return __uint_as_float(s[0]) + __uint_as_float(s[1]);
}
__device__ __forceinline__ float wave_sum(float x) {
  x += dppf<QP_XOR1>(x); x += dppf<QP_XOR2>(x); x += dppf<ROR4>(x); x += dppf<ROR8>(x);
  return xrow_sum(x);
}
template <int SRC, int DST> __device__ __forceinline__ unsigned bank_mov(unsigned old, unsigned x) {
  constexpr int sh = ((DST - SRC) & 3) * 4;
  constexpr int ctrl = sh == 0 ? QP_ID : (0x120 + sh);
  return (unsigned)__builtin_amdgcn_update_dpp((int)old, (int)x, ctrl, 0xf, 1 << DST, false);
}
__device__ __forceinline__ rsrc_t mkrs(const void* p, unsigned bytes) { return __builtin_amdgcn_make_buffer_rsrc((void*)p, 0, bytes, 0x00020000); }
__device__ __forceinline__ v2u trread(LAS unsigned char* p) {
  return __builtin_bit_cast(v2u, __builtin_amdgcn_ds_read_tr16_b64_v4i16((LAS s16x4*)p));
}
__device__ __forceinline__ v4u trfrag(LAS unsigned char* p, int rowstride4) {
  const v2u a = trread(p), b = trread(p + rowstride4);
  return v4u{a.x, a.y, b.x, b.y};
}
__device__ __forceinline__ v4u tagged(f32x4 v, v4u t) {
  return v4u{(__float_as_uint(v.x) & ~0x3ffu) | t.x, (__float_as_uint(v.y) & ~0x3ffu) | t.y, (__float_as_uint(v.z) & ~0x3ffu) | t.z, (__float_as_uint(v.w) & ~0x3ffu) | t.w};
}
__device__ __forceinline__ void untag(v4u& v, v4u t, unsigned& bad) { v ^= t; bad |= (v.x | v.y) | (v.z | v.w); }
__device__ __forceinline__ void lds_wait_ge(volatile LAS unsigned* p, unsigned v) {
  for (int s = 0; *p < v && s < LDS_SPIN_CAP; ++s) __builtin_amdgcn_s_sleep(1);
  CBAR();
}
__device__ __forceinline__ void lds_spin_ge(volatile LAS unsigned* p, unsigned v) {
  for (int s = 0; *p < v && s < LDS_SPIN_CAP; ++s) { }
  CBAR();
}
__device__ __forceinline__ void ln_row(const float (&v)[12], bool live, v2u (&out)[3]) {
  const float ref = __uint_as_float(__builtin_amdgcn_readfirstlane(__float_as_uint(v[0])));
  float s1 = 0.f, s2 = 0.f;
#pragma unroll
  for (int i = 0; i < 12; ++i) { const float d = live ? v[i] - ref : 0.f; s1 += d; s2 = fmaf(d, d, s2); }
  s1 = wave_sum(s1); s2 = wave_sum(s2);
  const float md = s1 * (1.f / 576.f);
  const float var = fmaxf(fmaf(-md, md, s2 * (1.f / 576.f)), 0.f);
  const float m = rbf(ref + md);
  const float den = rbf(__builtin_amdgcn_sqrtf(rbf(rbf(var) + 1e-5f)));
  const float inv = __builtin_amdgcn_rcpf(den);
#pragma unroll
  for (int i = 0; i < 3; ++i) {
    const unsigned a = cvtpk(v[4 * i] - m, v[4 * i + 1] - m), c = cvtpk(v[4 * i + 2] - m, v[4 * i + 3] - m);
    out[i] = v2u{cvtpk(bflo(a) * inv, bfhi(a) * inv), cvtpk(bflo(c) * inv, bfhi(c) * inv)};
  }
}

#define STAMP(i) do { } while (0)

__global__ void __launch_bounds__(512, 2) fused_block(const u16* __restrict__ x, const u16* __restrict__ Kc, const u16* __restrict__ Vc, const u16* __restrict__ wq,
    const u16* __restrict__ wo, const u16* __restrict__ wgt, const u16* __restrict__ wup, const u16* __restrict__ wdn, u16* out, unsigned char* ws) {
  __shared__ __attribute__((aligned(16))) unsigned char smem_[L_TOTAL];
  LAS unsigned char* L = (LAS unsigned char*)smem_;
  volatile LAS unsigned* FLG = (volatile LAS unsigned*)(L + L_FLG);
  const int tid = threadIdx.x, lane = tid & 63, w = __builtin_amdgcn_readfirstlane(tid >> 6);
  const unsigned pt0 = (unsigned)__builtin_amdgcn_s_memrealtime();
#define PROBE(k) do { if (PK == (k)) __hip_atomic_store((GAS unsigned*)(ws + (5 << 20) + blockIdx.x * 4), (k) < 2 ? pt0 : (unsigned)__builtin_amdgcn_s_memrealtime(), __ATOMIC_RELAXED, __HIP_MEMORY_SCOPE_AGENT); } while (0)
#define MKDESC(p, bytes) v4u{(unsigned)(u64)(p), (unsigned)((u64)(p) >> 32) & 0xffffu, (unsigned)(bytes), 0x00020000u}
  const unsigned lbase = (unsigned)(size_t)L;
#define DMA_ASMP(pol, desc, ldsa, voff, soff, imm) asm volatile("s_mov_b32 m0, %0\n\ts_nop 0\n\tbuffer_load_dwordx4 %1, %2, %3 offen offset:%4 " pol " lds" \
    :: "s"((ldsa) - (imm)), "v"(voff), "s"(desc), "s"(soff), "i"(imm) : "memory")
#define DMA_ASM(desc, ldsa, voff, soff, imm) DMA_ASMP(WPOL, desc, ldsa, voff, soff, imm)
  const bool live = lane < 48, two = (w == 2 || w == 7);
  const unsigned sw2 = (unsigned)((((lane & 7) ^ (((lane >> 4) & 1) << 1)) * 16) ^ (w << 6));
  v2u ep, xpk[3], xp2[3];
  if (w < 2) {
    {
      u64 pc; unsigned len;
      asm volatile("s_getpc_b64 %0\n.Lfb_code_begin:\n\ts_mov_b32 %1, .Lfb_code_end-.Lfb_code_begin" : "=s"(pc), "=s"(len));
      const v4u dc = MKDESC(pc, len);
      asm volatile("s_mov_b32 m0, %0\n\ts_nop 0\n\tbuffer_load_dword %1, %2, 0 offen lds\n\tbuffer_load_dword %3, %2, 0 offen lds"
        :: "s"(lbase + L_SCR + 2048), "v"(lane * 128 + w * 8192), "s"(dc), "v"(lane * 128 + w * 8192 + 16384) : "memory"); }
    const v4u dq = MKDESC(wq, 576 * 4096 * 2);
    const unsigned vo = (unsigned)((blockIdx.x & 3) * 144 + w * 8 + (lane >> 3)) * 8192u + (blockIdx.x >> 2) * 128 + sw2;
#pragma unroll
    for (int i = 0; i < 9; ++i) DMA_ASM(dq, lbase + L_WQ + w * 1024 + i * 2048, vo, i * 16 * 8192, 0);
    asm volatile("" : "=v"(ep), "=v"(xpk[0]), "=v"(xpk[1]), "=v"(xpk[2]), "=v"(xp2[0]), "=v"(xp2[1]), "=v"(xp2[2]));
  } else {
    const unsigned xo = (unsigned)(8 * (live ? lane : 47)), xo1 = xo + w * 1152, xo2 = xo + (w & 1) * 1152, z = 0;
    asm volatile("global_load_dwordx2 %0, %3, %4\n\tglobal_load_dwordx2 %1, %3, %4 offset:384\n\tglobal_load_dwordx2 %2, %3, %4 offset:768"
      : "=&v"(xpk[0]), "=&v"(xpk[1]), "=&v"(xpk[2]) : "v"(xo1), "s"(x) : "memory");
    asm volatile("global_load_dwordx2 %0, %1, %2 sc1" : "=&v"(ep) : "v"(z), "s"(ws) : "memory");
    if (two) asm volatile("global_load_dwordx2 %0, %3, %4\n\tglobal_load_dwordx2 %1, %3, %4 offset:384\n\tglobal_load_dwordx2 %2, %3, %4 offset:768"
      : "=&v"(xp2[0]), "=&v"(xp2[1]), "=&v"(xp2[2]) : "v"(xo2), "s"(x) : "memory");
    else asm volatile("" : "=v"(xp2[0]), "=v"(xp2[1]), "=v"(xp2[2]));
    if (w == 2) FLG[lane] = 0u;
  }
  WG_BARRIER();
  int wg = blockIdx.x;
  asm volatile("" : "+s"(wg));
  const int uu = MAPV == 0 ? wg : (MAPV == 1 ? (wg & 7) * 32 + (wg >> 3) : (MAPV == 2 ? ((wg * 37 + (wg >> 3) * 0) & 255) ^ ((wg >> 3) & 7) : (MAPV == 3 ? (wg ^ ((wg >> 3) & 7) ^ ((wg >> 6) & 3)) : (int)(__builtin_bitreverse32(wg) >> 24))));
  const int pair = uu >> 1, half = uu & 1, b = pair >> 4, kvh = pair & 15;
  const int rp = (pair & 7) * 16 + (pair >> 3);
  const int hsel = (pair >> 3) & 1;
  const bool holder = half == hsel;
  const bool isF = holder && rp < 72, isO = holder && rp >= 72 && rp < 108;
  const int fu = rp;
  const int sk = (rp - 72) & 3, sn = (rp - 72) >> 2;
  const int cb = wg >> 2, kq = wg & 3;
  const int n = lane & 15, g = lane >> 4, tq_ = (lane & 15) >> 2, tp = lane & 3;
  const int f2 = (tq_ >> 1) | ((g & 1) << 1);
  const rsrc_t rws = mkrs(ws, WS_END);
  const int dl = rp < 72 ? DELTA_F : (rp < 108 ? DELTA_O : 0);
  const int nt0 = 64 + (hsel ? dl : -dl);
  const int NT = half ? 128 - nt0 : nt0;
  const unsigned tsched = (unsigned)__builtin_amdgcn_s_memrealtime() + (unsigned)(T_TARGET - (isF ? LEAD_F : (isO ? LEAD_O : 0)) - NT * TAU);
  const unsigned tbase = ((unsigned)pair << 20) + (unsigned)(half * 64 + 64 - NT) * 8192u, xbase = ((unsigned)pair << 20) + (unsigned)((1 - half) * 64) * 8192u;
  const int voffK = (n >> 1) * 256 + (2 * g + (n & 1)) * 16;
  const int voffV = g * 2048 + n * 16;
  v4u kr[8], vr[8];
#define KOFF(i) (voffK + (((i) >> 1) & 1) * 2048 + ((i) & 1) * 128)
#define LDK(i, so) kr[i] = __builtin_amdgcn_raw_buffer_load_b128(rk, KOFF(i), (so) + ((i) >> 2) * 4096, KAUX)
#define LDV(i, so) vr[i] = __builtin_amdgcn_raw_buffer_load_b128(rv, voffV + (i) * 256, (so), VAUX)
  const rsrc_t rk = mkrs(Kc, 128u << 20), rv = mkrs(Vc, 128u << 20);
#define DMA_PACE() asm volatile("s_waitcnt vmcnt(" STR(DMA_INFL) ")" ::: "memory")
#define LDS_INC(f) do { if (lane == 0) __hip_atomic_fetch_add((LAS unsigned*)(FLG + (f)), 1u, __ATOMIC_RELAXED, __HIP_MEMORY_SCOPE_WORKGROUP); } while (0)
  const rsrc_t rx = mkrs(x, 8 * 576 * 2);
  u64 tag = 0;
  const unsigned guvo = (unsigned)(lane >> 3) * 9216u + fu * 128 + (((lane & 7) ^ ((lane >> 4) & 1)) * 16);
#define GU_DMA(i) do { const int mt_ = (i) >= 72, ri_ = (i) - 72 * mt_; const v4u dg_ = MKDESC(mt_ ? wup : wgt, 576 * 4608 * 2); \
    DMA_ASM(dg_, lbase + (i) * 1024, guvo ^ (unsigned)(((ri_ & 1) << 5) | (mt_ << 6)), ri_ * 8 * 9216, 0); } while (0)
  const int tl0 = isF ? N_EARLY * 1024 : (isO ? 131072 : 0);
  v4u de[8];
  const int dbk = w - 2;
#define DOWN_LOADS(arr, lo, hi) do { const rsrc_t rd = mkrs(wdn, dsz); \
    const unsigned vo_ = (dbk < 4 || n < 8) ? (unsigned)(fu * 64 + 8 * g) * 1152u + dbk * 256 + n * 16 : OOB; \
    _Pragma("unroll") for (int i = (lo); i < (hi); ++i) arr[i] = __builtin_amdgcn_raw_buffer_load_b128(rd, vo_, ((i >> 3) * 32 + (i & 7)) * 1152, WAUX); } while (0)
#define DOWN_TR(arr, o_) do { v4u t_[8]; \
      _Pragma("unroll") for (int jj = 0; jj < 8; ++jj) _Pragma("unroll") for (int mm = 0; mm < 4; ++mm) \
        t_[jj][mm] = __builtin_amdgcn_perm(arr[(o_) + 2 * mm + 1][jj >> 1], arr[(o_) + 2 * mm][jj >> 1], (jj & 1) ? 0x07060302u : 0x05040100u); \
      _Pragma("unroll") for (int jj = 0; jj < 8; ++jj) { arr[(o_) + jj] = t_[jj]; asm volatile("" : "+v"(arr[(o_) + jj])); } } while (0)

  const int nrounds = max((147456 - 4096 - tl0) >> 15, 0);
  if (w < 2) {
    static_assert(DMA_INFL == 12, "the wq signal sits behind the 12th piece");
    const int nw = isF ? N_EARLY : (isO ? 128 : 0);
#define LSTAMP() do { } while (0)
    LSTAMP();
#pragma unroll 1
    for (int run = 0; run < 2; ++run) {
      const int p0 = isF ? 72 * run : 64 * run, p1 = min(nw, isF ? p0 + 72 : p0 + 64);
      if (p1 <= p0) break;
      const v4u dw = isF ? MKDESC(run ? wup : wgt, 576 * 4608 * 2) : MKDESC(wo, 4096 * 576 * 2);
      const unsigned vo = isF ? (guvo ^ (unsigned)((w << 5) | (run << 6))) : (unsigned)(sk * 1024 + (lane >> 3)) * 1152u + sn * 128 + sw2;
      const unsigned sstep = isF ? 16u * 9216u : 16u * 1152u;
      unsigned so = (isF ? 0u : (unsigned)p0 * 8u * 1152u) + (sstep >> 1) * w, la = lbase + (p0 + w) * 1024;
      if (run) lds_wait_ge(FLG + F_QDONE, 4u);
#pragma unroll 1
      for (int i = p0; i < p1; i += 8) {
#pragma unroll
        for (int j = 0; j < 4; ++j) { DMA_ASM(dw, la, vo, so, 0); DMA_PACE(); la += 2048; so += sstep; }
        LSTAMP();
        if (i == 16) LDS_INC(F_XN);
      }
    }
    const v4u dk = MKDESC(Kc, 128u << 20), dv = MKDESC(Vc, 128u << 20);
#pragma unroll 1
    for (int rd = 0; rd < nrounds; ++rd) {
      const int slot = 2 * rd + w;
      if (tl0 + 4096 + (slot + 1) * 16384 > L_WQ) lds_wait_ge(FLG + F_QDONE, 4u);
      const unsigned so = ((PHASE && slot < NT - 64) ? XBASE : tbase) + (unsigned)slot * 8192u, la = lbase + tl0 + 4096 + slot * 16384;
#pragma unroll
      for (int i = 0; i < 8; ++i) { DMA_ASMP(KPOL, dk, la + i * 1024, voffK, so + (i >> 2) * 4096, ((i >> 1) & 1) * 2048 + (i & 1) * 128); DMA_PACE(); if ((i & 3) == 3) LSTAMP(); }
#pragma unroll
      for (int i = 0; i < 8; ++i) { DMA_ASMP(VPOL, dv, la + 8192 + i * 1024, voffV, so, i * 256); DMA_PACE(); if ((i & 3) == 3) LSTAMP();
        if (i == 3 && rd == 0 && nw == 0) LDS_INC(F_XN); }
      FLG[F_FULL + w] = rd;
    }
    SB();
#pragma unroll
    for (int i = 0; i < 8; ++i) { LDK(i, tbase + (2 * nrounds + w) * 8192); SB(); DMA_PACE(); if ((i & 3) == 3) LSTAMP(); }
#pragma unroll
    for (int i = 0; i < 8; ++i) { LDV(i, tbase + (2 * nrounds + w) * 8192); SB(); DMA_PACE(); if ((i & 3) == 3) LSTAMP(); }
    FLG[F_FULL + w] = nrounds;
  } else {
    if (two) asm volatile("s_waitcnt vmcnt(4)" ::: "memory"); else asm volatile("s_waitcnt vmcnt(1)" ::: "memory");
    asm volatile("" : "+v"(xpk[0]), "+v"(xpk[1]), "+v"(xpk[2]));
#define LN_TO_XN(src, row) do { float v[12]; \
      _Pragma("unroll") for (int i = 0; i < 3; ++i) { v[4 * i] = bflo(src[i].x); v[4 * i + 1] = bfhi(src[i].x); v[4 * i + 2] = bflo(src[i].y); v[4 * i + 3] = bfhi(src[i].y); } \
      v2u o3[3]; ln_row(v, live, o3); \
      if (live) { _Pragma("unroll") for (int i = 0; i < 3; ++i) *(LAS v2u*)(L + L_XN + (row) * XN_STR + (192 * i + 4 * lane) * 2) = o3[i]; } } while (0)
    LN_TO_XN(xpk, w);
    SB();
    asm volatile("s_waitcnt vmcnt(0)" : "+v"(ep), "+v"(xp2[0]), "+v"(xp2[1]), "+v"(xp2[2]) :: "memory");
    if (two) LN_TO_XN(xp2, w & 1);
    asm volatile("s_waitcnt lgkmcnt(0)" ::: "memory");
    LDS_INC(F_XN);
    tag = ((u64)(unsigned)__builtin_amdgcn_readfirstlane(ep.y) << 32 | (unsigned)__builtin_amdgcn_readfirstlane(ep.x)) + TAG_STEP;
    const v4u tg = TAG_FIELDS(tag);
    if (w == 7) { FLG[F_TAG] = (unsigned)tag; FLG[F_TAG + 1] = (unsigned)(tag >> 32); }
    const v4u tq4 = tg & 15u;
    if (!two) {
      lds_spin_ge(FLG + F_XN, 8u);
      const int tq = w - 3;
      v4u a[5], bb[5];
#pragma unroll
      for (int ks = 0; ks < 5; ++ks) {
        const int gg = ks < 4 ? g : (g & 1);
        a[ks] = trfrag(L + L_WQ + (32 * ks + 8 * gg + tq_) * 128 + ((tq ^ f2) * 32) + tp * 8, 4 * 128);
        bb[ks] = *(LAS v4u*)(L + L_XN + (n & 7) * XN_STR + (kq * 144 + 32 * ks + 8 * gg) * 2);
      }
      SB();
      if (g >= 2) bb[4] = v4u{0, 0, 0, 0};
      f32x4 d = {0.f, 0.f, 0.f, 0.f};
#pragma unroll
      for (int ks = 0; ks < 5; ++ks) d = mfma32(a[ks], bb[ks], d);
      __builtin_amdgcn_raw_buffer_store_b128((__builtin_bit_cast(v4u, d) & ~15u) | tq4, rws, n < 8 ? WS_QG + ((n * 4 + kq) * 4096 + cb * 64 + tq * 16 + 4 * g) * 4 : OOB, 0, AUX_SC1);
      LDS_INC(F_QDONE);
      STAMP(2);
    }
    if (w == 7) {
      const int qo = WS_QG + (b * 4 * 4096 + kvh * 256 + 4 * lane) * 4;
      v4u a[4];
      if (QPOLL_DELAY) __builtin_amdgcn_s_sleep(QPOLL_DELAY);
#pragma unroll 1
      for (int s = 0; s < SPIN_CAP; ++s) {
#pragma unroll
        for (int i = 0; i < 4; ++i) a[i] = __builtin_amdgcn_raw_buffer_load_b128(rws, qo, i * 16384, QPOLL_AUX);
        unsigned bad = 0;
#pragma unroll
        for (int i = 0; i < 4; ++i) untag(a[i], tq4, bad);
        bad |= dppu<QP_XOR1>(bad); bad |= dppu<QP_XOR2>(bad);
        if ((bad & 15u) == 0u) break;
        __builtin_amdgcn_s_sleep(2);
        CBAR();
      }
#define QV(p) __uint_as_float((p) | 8u)
#define QS(c) ((QV(a[0].c) + QV(a[1].c)) + (QV(a[2].c) + QV(a[3].c)))
      *(LAS v2u*)(L + L_Q + lane * 8) = v2u{cvtpk(QS(x), QS(y)), cvtpk(QS(z), QS(w))};
      asm volatile("s_waitcnt lgkmcnt(0)" ::: "memory");
      FLG[F_QREADY] = 1u;
      PROBE(0); PROBE(1); PROBE(2); PROBE(7);
      STAMP(2);
      {
        unsigned j0 = __builtin_amdgcn_raw_buffer_load_b32(rws, lane < 2 ? WS_O + lane * (1200 << 10) : OOB, 0, AUX_NT);
        unsigned j1 = __builtin_amdgcn_raw_buffer_load_b32(mkrs(out, 8 * 576 * 2), lane == 0 ? 0 : OOB, 0, AUX_NT);
        unsigned j2 = __builtin_amdgcn_raw_buffer_load_b32(mkrs(wdn, 4608 * 576 * 2), (isF && lane < 2) ? (fu * 64 + lane * 63) * 1152 : OOB, 0, AUX_NT);
        asm volatile("" :: "v"(j0), "v"(j1), "v"(j2)); }
    }
#pragma unroll
    for (int i = 0; i < 8; ++i) asm volatile("" : "=v"(kr[i]), "=v"(vr[i]));
  }
  if (w != 7) {
    lds_wait_ge(FLG + F_QREADY, 1u);
    STAMP(4);
    v4u qf[8];
    { const v4u qa0 = *(LAS v4u*)(L + L_Q + (((n >> 1) & 1) * 128 + 16 * g + 8 * (n & 1)) * 2), qa1 = *(LAS v4u*)(L + L_Q + (((n >> 1) & 1) * 128 + 64 + 16 * g + 8 * (n & 1)) * 2);
#pragma unroll
      for (int i = 0; i < 8; ++i) qf[i] = ((n >> 2) == (i >> 1)) ? ((i & 1) ? qa1 : qa0) : v4u{0, 0, 0, 0}; }
    const bool par = lane & 1;
    const unsigned jbmask = par ? 0xffff0000u : 0x0000ffffu;
    float m = -INFINITY, l = 0.f;
    f32x4 o = {0.f, 0.f, 0.f, 0.f};
    v4u pa0 = {0, 0, 0, 0}, pa1 = {0, 0, 0, 0};
#define PV(i, pa, xs_, MORE) { pa.x = bank_mov<((i) >> 1), 0>(pa.x, xs_); pa.y = bank_mov<((i) >> 1), 1>(pa.y, xs_); pa.z = bank_mov<((i) >> 1), 2>(pa.z, xs_); pa.w = bank_mov<((i) >> 1), 3>(pa.w, xs_); \
        o = mfma32(pa, vr[i], o); if (MORE) { WINA(); LDV(i, so); } SB(); }
#define BODY(MORE) { \
      f32x4 d = {0.f, 0.f, 0.f, 0.f}; \
      _Pragma("unroll") for (int i = 0; i < 8; ++i) { d = mfma32(qf[i], kr[i], d); if (MORE) { WINA(); LDK(i, so); } SB(); } \
      const float keep = par ? d.w : d.x, send = par ? d.y : d.z; \
      float s = keep + dppf<QP_XOR1>(send); \
      s = rbf(rbf(s) * 0.08838834764831845f); \
      if (__any(s > m)) { \
        float t = s; t = fmaxf(t, dppf<QP_XOR2>(t)); t = fmaxf(t, dppf<ROR4>(t)); t = fmaxf(t, dppf<ROR8>(t)); t = xrow_max(t); \
        const float mn = fmaxf(m, t); \
        const float al = __builtin_amdgcn_exp2f((m - mn) * LOG2E); \
        m = mn; l *= al; \
        const float a0 = dppf<QP_0000>(al), a1 = dppf<QP_1111>(al); \
        o.x *= a0; o.y *= a0; o.z *= a1; o.w *= a1; \
      } \
      const float p = __builtin_amdgcn_exp2f((s - m) * LOG2E); \
      l += p; \
      const unsigned p2 = cvtpk(p, p); \
      const unsigned x0 = dppu<QP_0011>(p2) & jbmask, x1 = dppu<QP_2233>(p2) & jbmask; \
      PV(0, pa0, x0, MORE) PV(1, pa1, x1, MORE) PV(2, pa0, x0, MORE) PV(3, pa1, x1, MORE) PV(4, pa0, x0, MORE) PV(5, pa1, x1, MORE) PV(6, pa0, x0, MORE) PV(7, pa1, x1, MORE) }
    if (w < 2) {
#pragma unroll 1
      for (int tnext = 2 * nrounds + 2 + w; tnext < NT; tnext += 2) { const unsigned so = tbase + (unsigned)tnext * 8192u;
        const int ahead = (int)((unsigned)__builtin_amdgcn_s_memrealtime() - tsched - (unsigned)(tnext * TAU));
#define WINA() do { WIN2(); asm volatile("s_cmp_ge_i32 %0, 0\n\ts_cbranch_scc1 1f\n\ts_waitcnt vmcnt(" STR(WINDOW) ")\n1:" :: "s"(ahead) : "memory", "scc"); } while (0)
        BODY(1)
      }
    }
#define REC_O(r) (*(LAS f32x4*)(L + L_REC + (r) * REC_STR + lane * 16))
#define REC_ML(r) (*(LAS f32x2_t*)(L + L_REC + (r) * REC_STR + 1024 + lane * 8))
#define MERGE(ro, rml) do { const float mn_ = fmaxf(m, rml.x), ms_ = mn_ == -INFINITY ? 0.f : mn_; \
      const float ea_ = __builtin_amdgcn_exp2f((m - ms_) * LOG2E), eb_ = __builtin_amdgcn_exp2f((rml.x - ms_) * LOG2E); \
      m = mn_; l = __fadd_rn(__fmul_rn(l, ea_), __fmul_rn(rml.y, eb_)); \
      const float a0_ = dppf<QP_0000>(ea_), a1_ = dppf<QP_1111>(ea_), b0_ = dppf<QP_0000>(eb_), b1_ = dppf<QP_1111>(eb_); \
      o.x = __fadd_rn(__fmul_rn(o.x, a0_), __fmul_rn(ro.x, b0_)); o.y = __fadd_rn(__fmul_rn(o.y, a0_), __fmul_rn(ro.y, b0_)); \
      o.z = __fadd_rn(__fmul_rn(o.z, a1_), __fmul_rn(ro.z, b1_)); o.w = __fadd_rn(__fmul_rn(o.w, a1_), __fmul_rn(ro.w, b1_)); } while (0)
    if (w == 0 && nrounds > 0) {
      lds_wait_ge(FLG + F_CHAIN, 5u);
      const f32x4 ro = REC_O(2); const f32x2_t rml = REC_ML(2); MERGE(ro, rml);
    }
    const u64 tagp = (u64)(unsigned)__builtin_amdgcn_readfirstlane(FLG[F_TAG + 1]) << 32 | (unsigned)__builtin_amdgcn_readfirstlane(FLG[F_TAG]);
#pragma unroll 1
    for (int t = w < 2 ? -1 : w - 2; t < 2 * nrounds; t += 5) {
      if (t >= 0) {
        lds_wait_ge(FLG + F_FULL + (t & 1), (unsigned)((t >> 1) + 1));
#pragma unroll
        for (int i = 0; i < 8; ++i) { kr[i] = *(LAS v4u*)(L + tl0 + 4096 + t * 16384 + i * 1024 + lane * 16); vr[i] = *(LAS v4u*)(L + tl0 + 4096 + t * 16384 + 8192 + i * 1024 + lane * 16); }
      }
      const unsigned so = 0; const int ahead = 0; BODY(0)
      if (t < 0) break;
    }
    STAMP(5);
    bool pub = true;
    if (w >= 2) {
      if (w == 2) lds_wait_ge(FLG + F_QDONE, 4u);
      else lds_wait_ge(FLG + F_CHAIN, (unsigned)(w - 2));
    } else {
      if (FLG[F_TICK] == 0u) {
        REC_O(w) = o; REC_ML(w) = f32x2_t{m, l};
        asm volatile("s_waitcnt lgkmcnt(0)" ::: "memory");
        unsigned tk = 0;
        if (lane == 0) tk = __hip_atomic_fetch_add((LAS unsigned*)(FLG + F_TICK), 1u, __ATOMIC_RELAXED, __HIP_MEMORY_SCOPE_WORKGROUP);
        pub = __builtin_amdgcn_readfirstlane(tk) != 0u;
      }
      CBAR();
    }
    if (pub) {
      if (w != 2) { const int r = w < 2 ? 1 - w : 2; const f32x4 ro = REC_O(r); const f32x2_t rml = REC_ML(r); MERGE(ro, rml); }
      if (w >= 2) { REC_O(2) = o; REC_ML(2) = f32x2_t{m, l}; asm volatile("s_waitcnt lgkmcnt(0)" ::: "memory"); FLG[F_CHAIN] = w - 1; }
      const rsrc_t rp_ = mkrs(ws, w < 2 ? WS_END : 0);
      l += dppf<QP_XOR2>(l); l += dppf<ROR4>(l); l += dppf<ROR8>(l); l = xrow_sum(l);
      const float inv = __builtin_amdgcn_rcpf(l), i0 = dppf<QP_0000>(inv), i1 = dppf<QP_1111>(inv);
      const int slot = kvh * 16 + b * 2 + half;
      const v2u pk = {cvtpk(o.x * i0, o.y * i0), cvtpk(o.z * i1, o.w * i1)};
      __builtin_amdgcn_raw_buffer_store_b64(pk, rp_, WS_O + kvh * 8192 + ((n >> 2) * 2 + (g >> 1)) * OSEG + (b * 2 + half) * OROW + (n & 3) * 16 + (g & 1) * 8, 0, AUX_SC1);
      unsigned ck = pk.x ^ pk.y;
      ck ^= dppu<QP_XOR1>(ck); ck ^= dppu<QP_XOR2>(ck); ck ^= dppu<HALF_MIRROR>(ck); ck = xrow_xor(ck);
      const unsigned lse0 = __float_as_uint(fmaf(m, LOG2E, __builtin_amdgcn_logf(l))), lse1 = dppu<QP_1111>(lse0);
      const unsigned salt = lse0 ^ __builtin_rotateleft32(lse1, 13) ^ (unsigned)(tagp >> 32);
      __builtin_amdgcn_raw_buffer_store_b128(v4u{lse0, lse1, ck ^ salt, dppu<ROR8>(ck) ^ salt}, rp_, lane == 0 ? WS_LSE + slot * LSE_STR : OOB, 0, AUX_SC1);
      if (w < 2) { PROBE(3); PROBE(5); }
    }
  }
  if (DE > 0) {
    if (isF && w >= 2 && w < 7) { const unsigned dsz = 4608 * 576 * 2; for (int z = 0; z < DE_DELAY * (w - 1); ++z) __builtin_amdgcn_s_sleep(127); DOWN_LOADS(de, 0, DE);
      if (DE == 8) DOWN_TR(de, 0);
#pragma unroll
      for (int i = 0; i < DE; ++i) asm volatile("" : "+v"(de[i])); }
    else {
#pragma unroll
      for (int i = 0; i < DE; ++i) asm volatile("" : "=v"(de[i])); }
  }
#pragma unroll 1
  for (int real = (w == 7) ? 0 : 1; real < 2; ++real) {
  const rsrc_t rt = mkrs(ws, real ? WS_END : 0);
  int lane_ = tid & 63;
  asm volatile("" : "+v"(lane_));
  const int lane = lane_, n = lane & 15, g = lane >> 4, tq_ = (lane & 15) >> 2, tp = lane & 3, f2 = (tq_ >> 1) | ((g & 1) << 1);
  const bool live = lane < 48;
  if (real) WG_BARRIER();
  STAMP(7);
  tag = real ? ((u64)(unsigned)__builtin_amdgcn_readfirstlane(FLG[F_TAG + 1]) << 32 | (unsigned)__builtin_amdgcn_readfirstlane(FLG[F_TAG])) : 0ull;
  const v4u tg = TAG_FIELDS(tag);
  const unsigned thint = (unsigned)(tag >> 32);
  if (isO) {
    v4u af[16];
#pragma unroll
    for (int i = 0; i < 16; ++i) af[i] = trfrag(L + L_WO + ((w >> 1) * 256 + (i >> 3) * 128 + (w & 1) * 64 + ((i >> 2) & 1) * 32 + 8 * g + tq_) * 128 + (((i & 3) ^ f2) * 32) + tp * 8, 4 * 128);
    const int kv_ = sk * 4 + (w >> 1);
    v4u of[4], ls;
#pragma unroll 1
    for (int s = 0; s < SPIN_CAP; ++s) {
      ls = __builtin_amdgcn_raw_buffer_load_b128(rt, WS_LSE + (kv_ * 16 + n) * LSE_STR, 0, AUX_SC1);
#pragma unroll
      for (int i = 0; i < 4; ++i) of[i] = __builtin_amdgcn_raw_buffer_load_b128(rt, WS_O + kv_ * 8192 + (w & 1) * 4 * OSEG + n * OROW + g * 16 + i * OSEG, 0, AUX_SC1);
      const v4u xx = (of[0] ^ of[1]) ^ (of[2] ^ of[3]);
      const unsigned ck = xrow_xor((xx.x ^ xx.y) ^ (xx.z ^ xx.w)) ^ ls.x ^ __builtin_rotateleft32(ls.y, 13) ^ thint ^ ((w & 1) ? ls.w : ls.z);
      if (ck == 0u) break;
      __builtin_amdgcn_s_sleep(1);
      CBAR();
    }
    { unsigned tk = 0;
      if (lane == 0 && real) tk = __hip_atomic_fetch_add((LAS unsigned*)(FLG + F_GOT), 1u, __ATOMIC_RELAXED, __HIP_MEMORY_SCOPE_WORKGROUP);
      if (sn == 0 && (__builtin_amdgcn_readfirstlane(tk) == 7u || !real)) {
#pragma unroll
        for (int i = 0; i < NHINT; ++i) __builtin_amdgcn_raw_buffer_store_b32(thint, rt, WS_DONE + lane * 128 + sk * 4, i * 8192, AUX_SC1); } }
    STAMP(9);
#define U2F __uint_as_float
    f32x4 d[8];
#pragma unroll
    for (int t = 0; t < 8; ++t) d[t] = f32x4{0.f, 0.f, 0.f, 0.f};
#pragma unroll
    for (int ks = 0; ks < 2; ++ks) {
      const v4u b0 = {of[2 * ks].x, of[2 * ks].z, of[2 * ks + 1].x, of[2 * ks + 1].z}, b1 = {of[2 * ks].y, of[2 * ks].w, of[2 * ks + 1].y, of[2 * ks + 1].w};
#pragma unroll
      for (int t = 0; t < 4; ++t) { d[t] = mfma32(af[ks * 4 + t], b0, d[t]); d[4 + t] = mfma32(af[8 + ks * 4 + t], b1, d[4 + t]); }
    }
    const float e0 = U2F(ls.x), e1 = U2F(ls.y);
    const float w0 = __builtin_amdgcn_rcpf(1.f + __builtin_amdgcn_exp2f(dppf<QP_XOR1>(e0) - e0)), w1 = __builtin_amdgcn_rcpf(1.f + __builtin_amdgcn_exp2f(dppf<QP_XOR1>(e1) - e1));
    {
      const bool od = n & 1;
      f32x4 r[4];
#pragma unroll
      for (int t = 0; t < 4; ++t) r[t] = w0 * d[t] + w1 * d[4 + t];
      f32x4 k0 = od ? r[2] : r[0], k1 = od ? r[3] : r[1], s0 = od ? r[0] : r[2], s1 = od ? r[1] : r[3];
      k0.x += dppf<QP_XOR1>(s0.x); k0.y += dppf<QP_XOR1>(s0.y); k0.z += dppf<QP_XOR1>(s0.z); k0.w += dppf<QP_XOR1>(s0.w);
      k1.x += dppf<QP_XOR1>(s1.x); k1.y += dppf<QP_XOR1>(s1.y); k1.z += dppf<QP_XOR1>(s1.z); k1.w += dppf<QP_XOR1>(s1.w);
      LAS unsigned char* dst = L + (real ? L_RED1 + w * 2048 : L_SCR) + (n >> 1) * 256 + (od ? 128 : 0) + g * 16;
      *(LAS f32x4*)dst = k0; *(LAS f32x4*)(dst + 64) = k1; }
    if (real) WG_BARRIER();
    {
      f32x4 t = *(LAS f32x4*)(L + L_RED1 + g * 4096 + w * 256 + n * 16) + *(LAS f32x4*)(L + L_RED1 + g * 4096 + 2048 + w * 256 + n * 16);
      t.x = xrow_sum(t.x); t.y = xrow_sum(t.y); t.z = xrow_sum(t.z); t.w = xrow_sum(t.w);
      __builtin_amdgcn_raw_buffer_store_b128(tagged(t, tg), rt, lane < 16 ? WS_H + ((w * 4 + sk) * 576 + sn * 64 + n * 4) * 4 : OOB, 0, AUX_SC1);
      STAMP(10);
    }
  } else if (isF) {
    v4u df[16];
#pragma unroll
    for (int i = 0; i < DE; ++i) df[i] = de[i];
    const bool dwave = (w >= 2 && w < 7) || !real;
    v2u xpk[3];
#pragma unroll
    for (int i = 0; i < 3; ++i) xpk[i] = __builtin_amdgcn_raw_buffer_load_b64(rx, 8 * (live ? lane : 47) + 384 * i, w * 1152, 0);
    SB();
    if (N_EARLY < 144 && real) {
#pragma unroll 1
      for (int i = N_EARLY + w; i < 144; i += 8) GU_DMA(i);
    }
    static_assert(N_EARLY == 144, "");
    if (dwave) { const unsigned dsz = real ? 4608 * 576 * 2 : 0; DOWN_LOADS(df, DE, 16); SB(); }
    else {
#pragma unroll
      for (int i = DE; i < 16; ++i) asm volatile("" : "=v"(df[i]));
    }
    if (N_EARLY < 144 && real) WG_BARRIER();
    SB();
    v4u gf[18];
    { LAS unsigned char* ga = L + ((tp & 1) ? L_UP : L_GATE) + (8 * g + tq_) * 128 + ((w ^ (f2 | ((tp & 1) << 2))) * 16) + (tp >> 1) * 8;
#pragma unroll
      for (int ks = 0; ks < 18; ++ks) gf[ks] = trfrag(ga + ks * 32 * 128, 4 * 128); }
#pragma unroll
    for (int ks = 0; ks < 18; ++ks) asm volatile("" : "+v"(gf[ks]));
    if (dwave) { if (DE != 8) DOWN_TR(df, 0); DOWN_TR(df, 8); }
    asm volatile("" : "+v"(xpk[0]), "+v"(xpk[1]), "+v"(xpk[2]));
    STAMP(6);
#define HINT_WAIT(idx) do { _Pragma("unroll 1") for (int s = 0; s < SPIN_CAP; ++s) { \
        const unsigned f0 = __builtin_amdgcn_raw_buffer_load_b32(rt, WS_DONE + (idx) * 128 + (lane & 3) * 4, 0, AUX_SC1); \
        if (f0 == thint) break; \
        __builtin_amdgcn_s_sleep(DONE_SLEEP); CBAR(); } } while (0)
    if (w == 7) { HINT_WAIT(fu); if (HP_DELAY) __builtin_amdgcn_s_sleep(HP_DELAY); }
    if (real) WG_BARRIER();
    int hoff[3];
#pragma unroll
    for (int i = 0; i < 3; ++i) { const int c = 24 * i + (lane >> 1); hoff[i] = (c & 3) * 288 + (c >> 2) * 16 + (lane & 1) * 8; asm volatile("" : "+v"(hoff[i])); }
    v4u hp[12];
#pragma unroll
    for (int i = 0; i < 12; ++i) asm volatile("" : "=v"(hp[i]));
    STAMP(3);
    unsigned pend = 15u;
#pragma unroll 1
    for (int s = 0; s < SPIN_CAP; ++s) {
#pragma unroll
      for (int q = 0; q < 4; ++q) if ((pend >> q) & 1u) {
#pragma unroll
        for (int i = 3 * q; i < 3 * q + 3; ++i) hp[i] = __builtin_amdgcn_raw_buffer_load_b128(rt, WS_H + (w * 4 * 576 + 4 * (live ? lane : 47)) * 4, i * 768, AUX_SC1); }
      SB();
#pragma unroll
      for (int q = 0; q < 4; ++q) if ((pend >> q) & 1u) {
        unsigned bad = 0;
#pragma unroll
        for (int i = 3 * q; i < 3 * q + 3; ++i) untag(hp[i], tg, bad);
        if (!__any((bad & 0x3ffu) != 0u)) pend &= ~(1u << q); }
      if (pend == 0u) break;
      CBAR();
    }
    STAMP(9);
    { float v[12];
#pragma unroll
      for (int i = 0; i < 3; ++i) {
        const f32x4 a = (__builtin_bit_cast(f32x4, hp[i]) + __builtin_bit_cast(f32x4, hp[3 + i])) + (__builtin_bit_cast(f32x4, hp[6 + i]) + __builtin_bit_cast(f32x4, hp[9 + i]));
        const unsigned r0 = cvtpk(a.x, a.y), r1 = cvtpk(a.z, a.w);
        const unsigned h0 = cvtpk(bflo(xpk[i].x) + bflo(r0), bfhi(xpk[i].x) + bfhi(r0)), h1 = cvtpk(bflo(xpk[i].y) + bflo(r1), bfhi(xpk[i].y) + bfhi(r1));
        v[4 * i] = bflo(h0); v[4 * i + 1] = bfhi(h0); v[4 * i + 2] = bflo(h1); v[4 * i + 3] = bfhi(h1); }
      v2u o3[3]; ln_row(v, live, o3);
      if (live) {
#pragma unroll
        for (int i = 0; i < 3; ++i) *(LAS v2u*)(L + (real ? L_XN + w * HN_STR : L_SCR) + hoff[i]) = o3[i]; } }
    if (real) WG_BARRIER();
    STAMP(11);
    {
      v4u hb[9];
#pragma unroll
      for (int ks = 0; ks < 9; ++ks) hb[ks] = *(LAS v4u*)(L + L_XN + (n & 7) * HN_STR + g * 288 + (n >> 3) * 144 + ks * 16);
      SB();
      f32x4 d = {0.f, 0.f, 0.f, 0.f}, d2 = {0.f, 0.f, 0.f, 0.f};
#pragma unroll
      for (int ks = 0; ks < 9; ++ks) { d = mfma32(gf[ks], hb[ks], d); d2 = mfma32(gf[9 + ks], hb[ks], d2); }
      d.x += dppf<ROR8>(d2.x); d.y += dppf<ROR8>(d2.y); d.z += dppf<ROR8>(d2.z); d.w += dppf<ROR8>(d2.w);
      auto s0 = __builtin_amdgcn_permlane16_swap(__float_as_uint(d.x), __float_as_uint(d.z), false, false);
      auto s1 = __builtin_amdgcn_permlane16_swap(__float_as_uint(d.y), __float_as_uint(d.w), false, false);
      float av[2];
#pragma unroll
      for (int e = 0; e < 2; ++e) {
        const float gg = rbf(__uint_as_float(e ? s1[0] : s0[0])), uu = rbf(__uint_as_float(e ? s1[1] : s0[1]));
        const float z2 = gg * fmaf(gg * gg, 0.044715f, 1.f) * (2.f * 0.7978845608028654f * LOG2E);
        const float ge = rbf(gg * __builtin_amdgcn_rcpf(1.f + __builtin_amdgcn_exp2f(-z2)));
        av[e] = ge * uu; }
      unsigned pk = cvtpk(av[0], av[1]);
      asm volatile("" : "+v"(pk));
      if (n < 8) *(LAS unsigned*)(L + L_ACT + n * ACT_STR + (8 * w + 2 * g) * 2) = pk; }
    if (real) WG_BARRIER();
    STAMP(12);
    if (dwave) {
      const v4u b0 = *(LAS v4u*)(L + L_ACT + (n & 7) * ACT_STR + g * 16), b1 = *(LAS v4u*)(L + L_ACT + (n & 7) * ACT_STR + 64 + g * 16);
      f32x4 d[8];
#pragma unroll
      for (int jj = 0; jj < 8; ++jj) d[jj] = mfma32(df[jj], b0, f32x4{0.f, 0.f, 0.f, 0.f});
#pragma unroll
      for (int jj = 0; jj < 8; ++jj) d[jj] = mfma32(df[8 + jj], b1, d[jj]);
      const bool hq = n >= 8;
      const unsigned po = (dbk < 4 || g < 2) ? WS_P + ((32 * dbk + 8 * g + (n >> 3)) * 72 + fu) * 128 + (n & 7) * 16 : OOB;
#pragma unroll
      for (int e = 0; e < 4; ++e)
        __builtin_amdgcn_raw_buffer_store_b128(tagged(f32x4{hq ? d[4][e] : d[0][e], hq ? d[5][e] : d[1][e], hq ? d[6][e] : d[2][e], hq ? d[7][e] : d[3][e]}, tg), rt, po, 2 * e * 72 * 128, AUX_SC1);
    }
  } else {
    const int unit = rp < 108 ? rp : 108 + (rp - 108) * 2 + half;
    if (unit < 144) {
      const int ob_ = lane & 7, oc = unit * 4;
      v2u xo = {0, 0};
      f32x4 a = {0.f, 0.f, 0.f, 0.f};
      if (w == 7) {
        xo = *(const GAS v2u*)(x + ob_ * 576 + oc);
        v4u hv;
        if (NHINT == 4) { HINT_WAIT(72 + unit); __builtin_amdgcn_s_sleep(RH_DELAY); }
#pragma unroll 1
        for (int s = 0; s < SPIN_CAP; ++s) {
          hv = __builtin_amdgcn_raw_buffer_load_b128(rt, WS_H + ((ob_ * 4 + ((lane >> 3) & 3)) * 576 + oc) * 4, 0, AUX_SC1);
          unsigned bad = 0; untag(hv, tg, bad);
          if ((bad & 0x3ffu) == 0u) break;
          __builtin_amdgcn_s_sleep(NHINT == 4 ? 4 : 16);
          CBAR();
        }
        a = __builtin_bit_cast(f32x4, hv);
        a.x += dppf<ROR8>(a.x); a.y += dppf<ROR8>(a.y); a.z += dppf<ROR8>(a.z); a.w += dppf<ROR8>(a.w);
        a.x = row2_sum(a.x); a.y = row2_sum(a.y); a.z = row2_sum(a.z); a.w = row2_sum(a.w);
        if (RP_DELAY) __builtin_amdgcn_s_sleep(RP_DELAY);
      }
      if (real) WG_BARRIER();
      STAMP(9);
      v4u p0, p1;
#pragma unroll 1
      for (int s = 0; s < SPIN_CAP; ++s) {
        p0 = __builtin_amdgcn_raw_buffer_load_b128(rt, WS_P + unit * 9216 + w * 1024 + lane * 16, 0, PPOLL_AUX);
        p1 = __builtin_amdgcn_raw_buffer_load_b128(rt, WS_P + unit * 9216 + 8192 + w * 128 + (lane & 7) * 16, 0, PPOLL_AUX);
        unsigned bad = 0; untag(p0, tg, bad); untag(p1, tg, bad);
        if ((bad & 0x3ffu) == 0u) break;
        __builtin_amdgcn_s_sleep(1);
        CBAR();
      }
      STAMP(11);
      f32x4 t = __builtin_bit_cast(f32x4, p0);
      t.x += dppf<ROR8>(t.x); t.y += dppf<ROR8>(t.y); t.z += dppf<ROR8>(t.z); t.w += dppf<ROR8>(t.w);
      t.x = xrow_sum(t.x); t.y = xrow_sum(t.y); t.z = xrow_sum(t.z); t.w = xrow_sum(t.w);
      if (lane < 8) *(LAS f32x4*)(L + L_RED + w * 128 + lane * 16) = t + __builtin_bit_cast(f32x4, p1);
      if (real) WG_BARRIER();
      if (w == 7 && lane < 8) {
        f32x4 f = *(LAS f32x4*)(L + L_RED + lane * 16);
#pragma unroll
        for (int r = 1; r < 8; ++r) f += *(LAS f32x4*)(L + L_RED + r * 128 + lane * 16);
        const unsigned r0 = cvtpk(a.x, a.y), r1 = cvtpk(a.z, a.w);
        const unsigned hh0 = cvtpk(bflo(xo.x) + bflo(r0), bfhi(xo.x) + bfhi(r0)), hh1 = cvtpk(bflo(xo.y) + bflo(r1), bfhi(xo.y) + bfhi(r1));
        const unsigned f0 = cvtpk(f.x, f.y), f1 = cvtpk(f.z, f.w);
        __builtin_amdgcn_raw_buffer_store_b64(v2u{cvtpk(bflo(hh0) + bflo(f0), bfhi(hh0) + bfhi(f0)), cvtpk(bflo(hh1) + bflo(f1), bfhi(hh1) + bfhi(f1))}, mkrs(out, real ? 8 * 576 * 2 : 0), (ob_ * 576 + oc) * 2, 0, 0);
        if (unit == 0 && lane == 0) __builtin_amdgcn_raw_buffer_store_b64(v2u{(unsigned)tag, (unsigned)(tag >> 32)}, rt, WS_EPOCH, 0, AUX_SC1);
        STAMP(15);
      }
      if (real && unit == 0 && w == 7) {
        const unsigned now = (unsigned)__builtin_amdgcn_s_memrealtime();
        int best = (PK == 0 || PK == 5 || PK == 7) ? 0x7fffffff : -0x7fffffff;
        for (int i = 0; i < 4; ++i) { const int v = (int)(__hip_atomic_load((GAS unsigned*)(ws + (5 << 20) + (i * 64 + lane) * 4), __ATOMIC_RELAXED, __HIP_MEMORY_SCOPE_AGENT) - now);
          best = (PK == 0 || PK == 5 || PK == 7) ? min(best, v) : max(best, v); }
        for (int o = 32; o > 0; o >>= 1) { const int v = __shfl_xor(best, o); best = (PK == 0 || PK == 5 || PK == 7) ? min(best, v) : max(best, v); }
        if (PK == 6) best = 0;
        while ((int)((unsigned)__builtin_amdgcn_s_memrealtime() - now) < best + 8000) __builtin_amdgcn_s_sleep(2);
      }
    }
  }
  }
  asm volatile(".Lfb_code_end:");
}

__global__ void tiny_first(unsigned char* ws) { if (threadIdx.x == 0) *(unsigned*)(ws + (6 << 20)) = (unsigned)__builtin_amdgcn_s_memrealtime(); }
__global__ void tiny_last(unsigned char* ws) { const unsigned t0 = *(volatile unsigned*)(ws + (5 << 20)); while ((int)((unsigned)__builtin_amdgcn_s_memrealtime() - t0) < 10000) __builtin_amdgcn_s_sleep(2); }
extern "C" void kernel_launch(void* const* d_in, const int* in_sizes, int n_in, void* d_out, int out_size, void* d_ws, size_t ws_size, hipStream_t stream) {
  tiny_first<<<1, 64, 0, stream>>>((unsigned char*)d_ws);
  fused_block<<<256, 512, 0, stream>>>((const u16*)d_in[0], (const u16*)d_in[1], (const u16*)d_in[2], (const u16*)d_in[3], (const u16*)d_in[4],
      (const u16*)d_in[6], (const u16*)d_in[7], (const u16*)d_in[8], (u16*)d_out, (unsigned char*)d_ws);
}
```

```cpp
#include <hip/hip_runtime.h>

typedef unsigned short u16;
typedef unsigned long long u64;
typedef unsigned v4u __attribute__((ext_vector_type(4)));
typedef unsigned v2u __attribute__((ext_vector_type(2)));
typedef float f32x4 __attribute__((ext_vector_type(4)));
typedef float f32x2 __attribute__((ext_vector_type(2)));
typedef short bf16x8 __attribute__((ext_vector_type(8)));
typedef short s16x4 __attribute__((ext_vector_type(4)));
typedef __bf16 bf16x2_t __attribute__((ext_vector_type(2)));
typedef __amdgpu_buffer_rsrc_t rsrc_t;
#define LAS __attribute__((address_space(3)))
#define STR2(x) #x
#define STR(x) STR2(x)

#define DELTA_F 5
#define DELTA_O 4
#define WINDOW 9
#define TAU 55
#define T_TARGET 3750
#define LEAD_F 60
#define QPOLL_DELAY 40
#define HP_DELAY 6
#define EARLY_FRAGS 1
#define DMA_INFL 12

constexpr int WS_EPOCH = 0;
constexpr int WS_LSE = 8192;
constexpr int WS_H = 16384;
constexpr int WS_HINT = 98304;
constexpr int WS_QG = 131072;
constexpr int WS_O = WS_QG + 8 * 4 * 4096 * 4;
constexpr int WS_P = WS_O + 16 * 8192;
constexpr int WS_END = WS_P + 144 * 72 * 128;
constexpr int L_GATE = 0, L_UP = 73728, L_WO = 0, L_TILE = 4096, L_RED1 = 131072, L_WQ = 129024;
constexpr int L_XN = 147456, XN_STR = 1168, HN_STR = 1216;
constexpr int L_REC = L_XN, REC_STR = 1536;
constexpr int L_ACT = L_XN + 8 * HN_STR, ACT_STR = 144;
constexpr int L_RED = L_ACT + 8 * ACT_STR;
constexpr int L_Q = L_RED + 1024;
constexpr int L_FLG = L_Q + 512;
constexpr int L_SINK = L_FLG + 256;
constexpr int L_TOTAL = L_SINK + 256;
static_assert(L_TOTAL <= 163840 && L_TOTAL > 81920, "one workgroup per CU");
constexpr int F_QREADY = 0, F_QDONE = 1, F_CHAIN = 2, F_TICK = 3, F_GOT = 4, F_XN = 5, F_FULL = 6, F_TAG = 8;

constexpr u64 TAG_STEP = 0x9E3779ull << 40 | 0x16Bull << 30 | 0x2D5ull << 20 | 0x0F3ull << 10 | 0x1B5ull;
#define TAG_FIELDS(t) v4u{(unsigned)(t) & 0x3ffu, ((unsigned)(t) >> 10) & 0x3ffu, ((unsigned)(t) >> 20) & 0x3ffu, (unsigned)((t) >> 30) & 0x3ffu}
constexpr int AUX_NT = 2, AUX_SC1 = 16;
constexpr unsigned OOB = 0x7FFFFFF0u;
constexpr int SPIN_CAP = 1 << 17, LDS_SPIN_CAP = 1 << 22;
constexpr float LOG2E = 1.4426950408889634f;

#define SB() __builtin_amdgcn_sched_barrier(0)
#define CBAR() asm volatile("" ::: "memory")
#define WG_BARRIER() asm volatile("s_waitcnt lgkmcnt(0)\n\ts_barrier" ::: "memory")
#define U2F __uint_as_float
#define F2U __float_as_uint

__device__ __forceinline__ f32x4 mfma32(v4u a, v4u b, f32x4 c) {
  return __builtin_amdgcn_mfma_f32_16x16x32_bf16(__builtin_bit_cast(bf16x8, a), __builtin_bit_cast(bf16x8, b), c, 0, 0, 0);
}
__device__ __forceinline__ unsigned cvtpk(float lo, float hi) { f32x2 v = {lo, hi}; return __builtin_bit_cast(unsigned, __builtin_convertvector(v, bf16x2_t)); }
__device__ __forceinline__ float rbf(float x) { return U2F(cvtpk(x, x) & 0xffff0000u); }
__device__ __forceinline__ float bflo(unsigned x) { return U2F(x << 16); }
__device__ __forceinline__ float bfhi(unsigned x) { return U2F(x & 0xffff0000u); }
template <int CTRL> __device__ __forceinline__ unsigned dppu(unsigned x) { return (unsigned)__builtin_amdgcn_mov_dpp((int)x, CTRL, 0xf, 0xf, true); }
template <int CTRL> __device__ __forceinline__ float dppf(float x) { return U2F(dppu<CTRL>(F2U(x))); }
constexpr int QP_XOR1 = 0xB1, QP_XOR2 = 0x4E, ROR4 = 0x124, ROR8 = 0x128, HALF_MIRROR = 0x141;
constexpr int QP_0011 = 0x50, QP_2233 = 0xFA, QP_0000 = 0x00, QP_1111 = 0x55, QP_ID = 0xE4;
__device__ __forceinline__ float xrow_max(float x) {
  auto s = __builtin_amdgcn_permlane16_swap(F2U(x), F2U(x), false, false);
  x = fmaxf(U2F(s[0]), U2F(s[1]));
  auto t = __builtin_amdgcn_permlane32_swap(F2U(x), F2U(x), false, false);
  return fmaxf(U2F(t[0]), U2F(t[1]));
}
__device__ __forceinline__ float row2_sum(float x) {
  auto s = __builtin_amdgcn_permlane16_swap(F2U(x), F2U(x), false, false);
  return U2F(s[0]) + U2F(s[1]);
}
__device__ __forceinline__ float xrow_sum(float x) {
  x = row2_sum(x);
  auto t = __builtin_amdgcn_permlane32_swap(F2U(x), F2U(x), false, false);
  return U2F(t[0]) + U2F(t[1]);
}
__device__ __forceinline__ unsigned xrow_xor(unsigned x) {
  auto s = __builtin_amdgcn_permlane16_swap(x, x, false, false);
  x = s[0] ^ s[1];
  auto t = __builtin_amdgcn_permlane32_swap(x, x, false, false);
  return t[0] ^ t[1];
}
__device__ __forceinline__ float wave_sum(float x) {
  x += dppf<QP_XOR1>(x); x += dppf<QP_XOR2>(x); x += dppf<ROR4>(x); x += dppf<ROR8>(x);
  return xrow_sum(x);
}
template <int SRC, int DST> __device__ __forceinline__ unsigned bank_mov(unsigned old, unsigned x) {
  constexpr int sh = ((DST - SRC) & 3) * 4;
  return (unsigned)__builtin_amdgcn_update_dpp((int)old, (int)x, sh == 0 ? QP_ID : (0x120 + sh), 0xf, 1 << DST, false);
}
__device__ __forceinline__ rsrc_t mkrs(const void* p, unsigned bytes) { return __builtin_amdgcn_make_buffer_rsrc((void*)p, 0, bytes, 0x00020000); }
__device__ __forceinline__ v4u trfrag(LAS unsigned char* p, int rows4) {
  const v2u a = __builtin_bit_cast(v2u, __builtin_amdgcn_ds_read_tr16_b64_v4i16((LAS s16x4*)p));
  const v2u b = __builtin_bit_cast(v2u, __builtin_amdgcn_ds_read_tr16_b64_v4i16((LAS s16x4*)(p + rows4)));
  return v4u{a.x, a.y, b.x, b.y};
}
__device__ __forceinline__ v4u tagged(f32x4 v, v4u t) { return (__builtin_bit_cast(v4u, v) & ~0x3ffu) | t; }
__device__ __forceinline__ void untag(v4u& v, v4u t, unsigned& bad) { v ^= t; bad |= (v.x | v.y) | (v.z | v.w); }
__device__ __forceinline__ void lds_wait_ge(volatile LAS unsigned* p, unsigned v) {
  for (int s = 0; *p < v && s < LDS_SPIN_CAP; ++s) __builtin_amdgcn_s_sleep(1);
  CBAR();
}
__device__ __forceinline__ void lds_spin_ge(volatile LAS unsigned* p, unsigned v) {
  for (int s = 0; *p < v && s < LDS_SPIN_CAP; ++s) { }
  CBAR();
}
__device__ __forceinline__ void ln_row(const float (&v)[12], bool live, v2u (&out)[3]) {
  const float ref = U2F(__builtin_amdgcn_readfirstlane(F2U(v[0])));
  float s1 = 0.f, s2 = 0.f, t1 = 0.f, t2 = 0.f;
#pragma unroll
  for (int i = 0; i < 12; i += 2) { const float d = v[i] - ref, e = v[i + 1] - ref; s1 += d; s2 = fmaf(d, d, s2); t1 += e; t2 = fmaf(e, e, t2); }
  s1 = wave_sum(live ? s1 + t1 : 0.f); s2 = wave_sum(live ? s2 + t2 : 0.f);
  const float md = s1 * (1.f / 576.f);
  const float var = fmaxf(fmaf(-md, md, s2 * (1.f / 576.f)), 0.f);
  const float m = rbf(ref + md);
  const float inv = __builtin_amdgcn_rcpf(rbf(__builtin_amdgcn_sqrtf(rbf(rbf(var) + 1e-5f))));
#pragma unroll
  for (int i = 0; i < 3; ++i) {
    const unsigned a = cvtpk(v[4 * i] - m, v[4 * i + 1] - m), c = cvtpk(v[4 * i + 2] - m, v[4 * i + 3] - m);
    out[i] = v2u{cvtpk(bflo(a) * inv, bfhi(a) * inv), cvtpk(bflo(c) * inv, bfhi(c) * inv)};
  }
}

__global__ void __launch_bounds__(512, 2) fused_block(const u16* __restrict__ x, const u16* __restrict__ Kc, const u16* __restrict__ Vc, const u16* __restrict__ wq,
    const u16* __restrict__ wo, const u16* __restrict__ wgt, const u16* __restrict__ wup, const u16* __restrict__ wdn, u16* out, unsigned char* ws) {
  __shared__ __attribute__((aligned(16))) unsigned char smem_[L_TOTAL];
  LAS unsigned char* L = (LAS unsigned char*)smem_;
  volatile LAS unsigned* FLG = (volatile LAS unsigned*)(L + L_FLG);
  const int tid = threadIdx.x, lane = tid & 63, w = __builtin_amdgcn_readfirstlane(tid >> 6);
#define MKDESC(p, bytes) v4u{(unsigned)(u64)(p), (unsigned)((u64)(p) >> 32) & 0xffffu, (unsigned)(bytes), 0x00020000u}
  const unsigned lbase = (unsigned)(size_t)L;
#define DMA_ASM(desc, ldsa, voff, soff, imm) asm volatile("s_mov_b32 m0, %0\n\ts_nop 0\n\tbuffer_load_dwordx4 %1, %2, %3 offen offset:%4 nt lds" \
    :: "s"((ldsa) - (imm)), "v"(voff), "s"(desc), "s"(soff), "i"(imm) : "memory")
#define DMA_PACE() asm volatile("s_waitcnt vmcnt(" STR(DMA_INFL) ")" ::: "memory")
#define LDS_INC(f) do { if (lane == 0) __hip_atomic_fetch_add((LAS unsigned*)(FLG + (f)), 1u, __ATOMIC_RELAXED, __HIP_MEMORY_SCOPE_WORKGROUP); } while (0)
  const bool live = lane < 48, two = (w == 2 || w == 7);
  const unsigned sw2 = (unsigned)((((lane & 7) ^ (((lane >> 4) & 1) << 1)) * 16) ^ (w << 6));
  v2u ep, xpk[3], xp2[3];
  if (w < 2) {
    {
      u64 pc; unsigned len;
      asm volatile("s_getpc_b64 %0\n.Lfb_code_begin:\n\ts_mov_b32 %1, .Lfb_code_end-.Lfb_code_begin" : "=s"(pc), "=s"(len));
      const v4u dc = MKDESC(pc, len);
      asm volatile("s_mov_b32 m0, %0\n\ts_nop 0\n\tbuffer_load_dword %1, %2, 0 offen lds\n\tbuffer_load_dword %3, %2, 0 offen lds"
        :: "s"(lbase + L_SINK), "v"(lane * 128 + w * 8192), "s"(dc), "v"(lane * 128 + w * 8192 + 16384) : "memory"); }
    const v4u dq = MKDESC(wq, 576 * 4096 * 2);
    const unsigned vo = (unsigned)((blockIdx.x & 3) * 144 + w * 8 + (lane >> 3)) * 8192u + (blockIdx.x >> 2) * 128 + sw2;
#pragma unroll
    for (int i = 0; i < 9; ++i) DMA_ASM(dq, lbase + L_WQ + w * 1024 + i * 2048, vo, i * 16 * 8192, 0);
    asm volatile("" : "=v"(ep), "=v"(xpk[0]), "=v"(xpk[1]), "=v"(xpk[2]), "=v"(xp2[0]), "=v"(xp2[1]), "=v"(xp2[2]));
  } else {
    const unsigned xo = (unsigned)(8 * (live ? lane : 47)), xo1 = xo + w * 1152, xo2 = xo + (w & 1) * 1152, z = 0;
    asm volatile("global_load_dwordx2 %0, %3, %4\n\tglobal_load_dwordx2 %1, %3, %4 offset:384\n\tglobal_load_dwordx2 %2, %3, %4 offset:768"
      : "=&v"(xpk[0]), "=&v"(xpk[1]), "=&v"(xpk[2]) : "v"(xo1), "s"(x) : "memory");
    asm volatile("global_load_dwordx2 %0, %1, %2 sc1" : "=&v"(ep) : "v"(z), "s"(ws) : "memory");
    if (two) asm volatile("global_load_dwordx2 %0, %3, %4\n\tglobal_load_dwordx2 %1, %3, %4 offset:384\n\tglobal_load_dwordx2 %2, %3, %4 offset:768"
      : "=&v"(xp2[0]), "=&v"(xp2[1]), "=&v"(xp2[2]) : "v"(xo2), "s"(x) : "memory");
    else asm volatile("" : "=v"(xp2[0]), "=v"(xp2[1]), "=v"(xp2[2]));
    if (w == 2) FLG[lane] = 0u;
  }
  WG_BARRIER();
  int wg = blockIdx.x;
  asm volatile("" : "+s"(wg));
  const int pair = wg >> 1, half = wg & 1, b = pair >> 4, kvh = pair & 15;
  const int rp = (pair & 7) * 16 + (pair >> 3);
  const bool holder = half == ((pair >> 3) & 1);
  const bool isF = holder && rp < 72, isO = holder && rp >= 72 && rp < 108;
  const int fu = rp;
  const int sk = (rp - 72) & 3, sn = (rp - 72) >> 2;
  const int cb = wg >> 2, kq = wg & 3;
  const int n = lane & 15, g = lane >> 4, tq_ = (lane & 15) >> 2, tp = lane & 3;
  const int f2 = (tq_ >> 1) | ((g & 1) << 1);
  const rsrc_t rws = mkrs(ws, WS_END);
  const int dl = rp < 72 ? DELTA_F : (rp < 108 ? DELTA_O : 0);
  const int NT = holder ? 64 - dl : 64 + dl;
  const unsigned tsched = (unsigned)__builtin_amdgcn_s_memrealtime() + (unsigned)(T_TARGET - (isF ? LEAD_F : 0) - NT * TAU);
  const unsigned tbase = ((unsigned)pair << 20) + (unsigned)(half * 64 + 64 - NT) * 8192u, xbase = ((unsigned)pair << 20) + (unsigned)((1 - half) * 64) * 8192u;
  const int voffK = (n >> 1) * 256 + (2 * g + (n & 1)) * 16;
  const int voffV = g * 2048 + n * 16;
  v4u kr[8], vr[8];
#define LDK(i, so) kr[i] = __builtin_amdgcn_raw_buffer_load_b128(rk, voffK + (((i) >> 1) & 1) * 2048 + ((i) & 1) * 128, (so) + ((i) >> 2) * 4096, AUX_NT)
#define LDV(i, so) vr[i] = __builtin_amdgcn_raw_buffer_load_b128(rv, voffV + (i) * 256, (so), AUX_NT)
  const rsrc_t rk = mkrs(Kc, 128u << 20), rv = mkrs(Vc, 128u << 20);
  const int nrounds = (isF || isO) ? 0 : 4;

  if (w < 2) {
    if (nrounds == 0) {
#pragma unroll 1
      for (int run = 0; run < 2; ++run) {
        const int p0 = isF ? 72 * run : 64 * run, p1 = isF ? p0 + 72 : p0 + 64;
        const v4u dw = isF ? MKDESC(run ? wup : wgt, 576 * 4608 * 2) : MKDESC(wo, 4096 * 576 * 2);
        const unsigned vo = isF ? (unsigned)(lane >> 3) * 9216u + fu * 128 + (((lane & 7) ^ ((lane >> 4) & 1) ^ (w << 1) ^ (run << 2)) * 16)
                                : (unsigned)(sk * 1024 + (lane >> 3)) * 1152u + sn * 128 + sw2;
        const unsigned sstep = isF ? 16u * 9216u : 16u * 1152u;
        unsigned so = (isF ? 0u : (unsigned)p0 * 8u * 1152u) + (sstep >> 1) * w, la = lbase + (p0 + w) * 1024;
        if (run) lds_wait_ge(FLG + F_QDONE, 4u);
#pragma unroll 1
        for (int i = p0; i < p1; i += 8) {
#pragma unroll
          for (int j = 0; j < 4; ++j) { DMA_ASM(dw, la, vo, so, 0); DMA_PACE(); la += 2048; so += sstep; }
          if (i == 16) LDS_INC(F_XN);
        }
      }
    } else {
      const v4u dk = MKDESC(Kc, 128u << 20), dv = MKDESC(Vc, 128u << 20);
#pragma unroll 1
      for (int rd = 0; rd < 4; ++rd) {
        const int slot = 2 * rd + w;
        if (slot == 7) lds_wait_ge(FLG + F_QDONE, 4u);
        const unsigned so = (slot < NT - 64 ? xbase : tbase) + (unsigned)slot * 8192u, la = lbase + L_TILE + slot * 16384;
#pragma unroll
        for (int i = 0; i < 8; ++i) { DMA_ASM(dk, la + i * 1024, voffK, so + (i >> 2) * 4096, ((i >> 1) & 1) * 2048 + (i & 1) * 128); DMA_PACE(); }
#pragma unroll
        for (int i = 0; i < 8; ++i) { DMA_ASM(dv, la + 8192 + i * 1024, voffV, so, i * 256); DMA_PACE(); if (i == 3 && rd == 0) LDS_INC(F_XN); }
        FLG[F_FULL + w] = rd;
      }
    }
    SB();
#pragma unroll
    for (int i = 0; i < 8; ++i) { LDK(i, tbase + (2 * nrounds + w) * 8192); SB(); DMA_PACE(); }
#pragma unroll
    for (int i = 0; i < 8; ++i) { LDV(i, tbase + (2 * nrounds + w) * 8192); SB(); DMA_PACE(); }
    FLG[F_FULL + w] = 4u;
  } else {
    if (two) asm volatile("s_waitcnt vmcnt(4)" ::: "memory"); else asm volatile("s_waitcnt vmcnt(1)" ::: "memory");
    asm volatile("" : "+v"(xpk[0]), "+v"(xpk[1]), "+v"(xpk[2]));
#define LN_TO_XN(src, row) do { float v[12]; \
      _Pragma("unroll") for (int i = 0; i < 3; ++i) { v[4 * i] = bflo(src[i].x); v[4 * i + 1] = bfhi(src[i].x); v[4 * i + 2] = bflo(src[i].y); v[4 * i + 3] = bfhi(src[i].y); } \
      v2u o3[3]; ln_row(v, live, o3); \
      if (live) { _Pragma("unroll") for (int i = 0; i < 3; ++i) *(LAS v2u*)(L + L_XN + (row) * XN_STR + (192 * i + 4 * lane) * 2) = o3[i]; } } while (0)
    LN_TO_XN(xpk, w);
    SB();
    asm volatile("s_waitcnt vmcnt(0)" : "+v"(ep), "+v"(xp2[0]), "+v"(xp2[1]), "+v"(xp2[2]) :: "memory");
    if (two) LN_TO_XN(xp2, w & 1);
    asm volatile("s_waitcnt lgkmcnt(0)" ::: "memory");
    LDS_INC(F_XN);
    const u64 tag = ((u64)(unsigned)__builtin_amdgcn_readfirstlane(ep.y) << 32 | (unsigned)__builtin_amdgcn_readfirstlane(ep.x)) + TAG_STEP;
    const v4u tq4 = TAG_FIELDS(tag) & 15u;
    if (!two) {
      lds_spin_ge(FLG + F_XN, 8u);
      const int tq = w - 3;
      v4u a[5], bb[5];
#pragma unroll
      for (int ks = 0; ks < 5; ++ks) {
        const int gg = ks < 4 ? g : (g & 1);
        a[ks] = trfrag(L + L_WQ + (32 * ks + 8 * gg + tq_) * 128 + ((tq ^ f2) * 32) + tp * 8, 4 * 128);
        bb[ks] = *(LAS v4u*)(L + L_XN + (n & 7) * XN_STR + (kq * 144 + 32 * ks + 8 * gg) * 2);
      }
      SB();
      if (g >= 2) bb[4] = v4u{0, 0, 0, 0};
      f32x4 d = {0.f, 0.f, 0.f, 0.f};
#pragma unroll
      for (int ks = 0; ks < 5; ++ks) d = mfma32(a[ks], bb[ks], d);
      __builtin_amdgcn_raw_buffer_store_b128((__builtin_bit_cast(v4u, d) & ~15u) | tq4, rws, n < 8 ? WS_QG + ((n * 4 + kq) * 4096 + cb * 64 + tq * 16 + 4 * g) * 4 : OOB, 0, AUX_SC1);
      LDS_INC(F_QDONE);
    }
    if (w == 7) {
      FLG[F_TAG] = (unsigned)tag; FLG[F_TAG + 1] = (unsigned)(tag >> 32);
      const int qo = WS_QG + (b * 4 * 4096 + kvh * 256 + 4 * lane) * 4;
      v4u a[4];
      __builtin_amdgcn_s_sleep(QPOLL_DELAY);
#pragma unroll 1
      for (int s = 0; s < SPIN_CAP; ++s) {
#pragma unroll
        for (int i = 0; i < 4; ++i) a[i] = __builtin_amdgcn_raw_buffer_load_b128(rws, qo, i * 16384, AUX_SC1);
        unsigned bad = 0;
#pragma unroll
        for (int i = 0; i < 4; ++i) untag(a[i], tq4, bad);
        bad |= dppu<QP_XOR1>(bad); bad |= dppu<QP_XOR2>(bad);
        if ((bad & 15u) == 0u) break;
        __builtin_amdgcn_s_sleep(2);
        CBAR();
      }
#define QS(c) ((U2F(a[0].c | 8u) + U2F(a[1].c | 8u)) + (U2F(a[2].c | 8u) + U2F(a[3].c | 8u)))
      *(LAS v2u*)(L + L_Q + lane * 8) = v2u{cvtpk(QS(x), QS(y)), cvtpk(QS(z), QS(w))};
      asm volatile("s_waitcnt lgkmcnt(0)" ::: "memory");
      FLG[F_QREADY] = 1u;
    }
#pragma unroll
    for (int i = 0; i < 8; ++i) asm volatile("" : "=v"(kr[i]), "=v"(vr[i]));
  }
  lds_wait_ge(FLG + F_QREADY, 1u);
  const u64 tag = (u64)(unsigned)__builtin_amdgcn_readfirstlane(FLG[F_TAG + 1]) << 32 | (unsigned)__builtin_amdgcn_readfirstlane(FLG[F_TAG]);
  const unsigned thint = (unsigned)(tag >> 32);
  if (w < 2 || (w < 7 && nrounds > 0)) {
    v4u qf[8];
    { const v4u qa0 = *(LAS v4u*)(L + L_Q + (((n >> 1) & 1) * 128 + 16 * g + 8 * (n & 1)) * 2), qa1 = *(LAS v4u*)(L + L_Q + (((n >> 1) & 1) * 128 + 64 + 16 * g + 8 * (n & 1)) * 2);
#pragma unroll
      for (int i = 0; i < 8; ++i) qf[i] = ((n >> 2) == (i >> 1)) ? ((i & 1) ? qa1 : qa0) : v4u{0, 0, 0, 0}; }
    const bool par = lane & 1;
    const unsigned jbmask = par ? 0xffff0000u : 0x0000ffffu;
    float m = -INFINITY, l = 0.f;
    f32x4 o = {0.f, 0.f, 0.f, 0.f};
    v4u pa0 = {0, 0, 0, 0}, pa1 = {0, 0, 0, 0};
#define WINA() asm volatile("s_cmp_ge_i32 %0, 0\n\ts_cbranch_scc1 1f\n\ts_waitcnt vmcnt(" STR(WINDOW) ")\n1:" :: "s"(ahead) : "memory", "scc")
#define PV(i, pa, xs_, MORE) { pa.x = bank_mov<((i) >> 1), 0>(pa.x, xs_); pa.y = bank_mov<((i) >> 1), 1>(pa.y, xs_); pa.z = bank_mov<((i) >> 1), 2>(pa.z, xs_); pa.w = bank_mov<((i) >> 1), 3>(pa.w, xs_); \
        o = mfma32(pa, vr[i], o); if (MORE) { WINA(); LDV(i, so); } SB(); }
#define BODY(MORE) { \
      f32x4 d = {0.f, 0.f, 0.f, 0.f}; \
      _Pragma("unroll") for (int i = 0; i < 8; ++i) { d = mfma32(qf[i], kr[i], d); if (MORE) { WINA(); LDK(i, so); } SB(); } \
      const float keep = par ? d.w : d.x, send = par ? d.y : d.z; \
      float s = keep + dppf<QP_XOR1>(send); \
      s = rbf(rbf(s) * 0.08838834764831845f); \
      if (__any(s > m)) { \
        float t = s; t = fmaxf(t, dppf<QP_XOR2>(t)); t = fmaxf(t, dppf<ROR4>(t)); t = fmaxf(t, dppf<ROR8>(t)); t = xrow_max(t); \
        const float mn = fmaxf(m, t); \
        const float al = __builtin_amdgcn_exp2f((m - mn) * LOG2E); \
        m = mn; l *= al; \
        const float a0 = dppf<QP_0000>(al), a1 = dppf<QP_1111>(al); \
        o.x *= a0; o.y *= a0; o.z *= a1; o.w *= a1; \
      } \
      const float p = __builtin_amdgcn_exp2f((s - m) * LOG2E); \
      l += p; \
      const unsigned p2 = cvtpk(p, p); \
      const unsigned x0 = dppu<QP_0011>(p2) & jbmask, x1 = dppu<QP_2233>(p2) & jbmask; \
      PV(0, pa0, x0, MORE) PV(1, pa1, x1, MORE) PV(2, pa0, x0, MORE) PV(3, pa1, x1, MORE) PV(4, pa0, x0, MORE) PV(5, pa1, x1, MORE) PV(6, pa0, x0, MORE) PV(7, pa1, x1, MORE) }
    if (w < 2) {
#pragma unroll 1
      for (int tnext = 2 * nrounds + 2 + w; tnext < NT; tnext += 2) {
        const unsigned so = tbase + (unsigned)tnext * 8192u;
        const int ahead = (int)((unsigned)__builtin_amdgcn_s_memrealtime() - tsched - (unsigned)(tnext * TAU));
        BODY(1)
      }
    }
#define REC_O(r) (*(LAS f32x4*)(L + L_REC + (r) * REC_STR + lane * 16))
#define REC_ML(r) (*(LAS f32x2*)(L + L_REC + (r) * REC_STR + 1024 + lane * 8))
#define MERGE(r) do { const f32x4 ro = REC_O(r); const f32x2 rml = REC_ML(r); const float mn_ = fmaxf(m, rml.x); \
      const float ea_ = __builtin_amdgcn_exp2f((m - mn_) * LOG2E), eb_ = __builtin_amdgcn_exp2f((rml.x - mn_) * LOG2E); \
      const float a0_ = dppf<QP_0000>(ea_), a1_ = dppf<QP_1111>(ea_), b0_ = dppf<QP_0000>(eb_), b1_ = dppf<QP_1111>(eb_); \
      float la_ = l * ea_, lb_ = rml.y * eb_; f32x4 oa_ = {o.x * a0_, o.y * a0_, o.z * a1_, o.w * a1_}, ob_ = {ro.x * b0_, ro.y * b0_, ro.z * b1_, ro.w * b1_}; \
      asm volatile("" : "+v"(la_), "+v"(lb_), "+v"(oa_), "+v"(ob_)); \
      m = mn_; l = la_ + lb_; o = oa_ + ob_; } while (0)
    if (w == 0 && nrounds > 0) { lds_wait_ge(FLG + F_CHAIN, 5u); MERGE(2); }
#pragma unroll 1
    for (int t = w < 2 ? -1 : w - 2; t < 8; t += 5) {
      if (t >= 0) {
        lds_wait_ge(FLG + F_FULL + (t & 1), (unsigned)((t >> 1) + 1));
#pragma unroll
        for (int i = 0; i < 8; ++i) { kr[i] = *(LAS v4u*)(L + L_TILE + t * 16384 + i * 1024 + lane * 16); vr[i] = *(LAS v4u*)(L + L_TILE + t * 16384 + 8192 + i * 1024 + lane * 16); }
      }
      const unsigned so = 0; const int ahead = 0; BODY(0)
      if (t < 0) break;
    }
    if (w >= 2) {
      if (w == 2) lds_wait_ge(FLG + F_QDONE, 4u);
      else { lds_wait_ge(FLG + F_CHAIN, (unsigned)(w - 2)); MERGE(2); }
      REC_O(2) = o; REC_ML(2) = f32x2{m, l};
      asm volatile("s_waitcnt lgkmcnt(0)" ::: "memory");
      FLG[F_CHAIN] = w - 1;
    } else {
      bool pub = true;
      if (FLG[F_TICK] == 0u) {
        REC_O(w) = o; REC_ML(w) = f32x2{m, l};
        asm volatile("s_waitcnt lgkmcnt(0)" ::: "memory");
        unsigned tk = 0;
        if (lane == 0) tk = __hip_atomic_fetch_add((LAS unsigned*)(FLG + F_TICK), 1u, __ATOMIC_RELAXED, __HIP_MEMORY_SCOPE_WORKGROUP);
        pub = __builtin_amdgcn_readfirstlane(tk) != 0u;
      }
      CBAR();
      if (pub) {
        MERGE(1 - w);
        l += dppf<QP_XOR2>(l); l += dppf<ROR4>(l); l += dppf<ROR8>(l); l = xrow_sum(l);
        const float inv = __builtin_amdgcn_rcpf(l), i0 = dppf<QP_0000>(inv), i1 = dppf<QP_1111>(inv);
        const v2u pk = {cvtpk(o.x * i0, o.y * i0), cvtpk(o.z * i1, o.w * i1)};
        __builtin_amdgcn_raw_buffer_store_b64(pk, rws, WS_O + kvh * 8192 + ((n >> 2) * 2 + (g >> 1)) * 1024 + (b * 2 + half) * 64 + (n & 3) * 16 + (g & 1) * 8, 0, AUX_SC1);
        unsigned ck = pk.x ^ pk.y;
        ck ^= dppu<QP_XOR1>(ck); ck ^= dppu<QP_XOR2>(ck); ck ^= dppu<HALF_MIRROR>(ck); ck = xrow_xor(ck);
        const unsigned lse0 = F2U(fmaf(m, LOG2E, __builtin_amdgcn_logf(l))), lse1 = dppu<QP_1111>(lse0);
        const unsigned salt = lse0 ^ __builtin_rotateleft32(lse1, 13) ^ thint;
        __builtin_amdgcn_raw_buffer_store_b128(v4u{lse0, lse1, ck ^ salt, dppu<ROR8>(ck) ^ salt}, rws, lane == 0 ? WS_LSE + (kvh * 16 + b * 2 + half) * 16 : OOB, 0, AUX_SC1);
      }
    }
  }
  const v4u tg = TAG_FIELDS(tag);
#define WEIGHTS_LANDED() do { lds_wait_ge(FLG + F_FULL, 4u); lds_wait_ge(FLG + F_FULL + 1, 4u); } while (0)
  if (isO) {
    if (EARLY_FRAGS && w >= 2) WEIGHTS_LANDED(); else WG_BARRIER();
    v4u af[16];
#pragma unroll
    for (int i = 0; i < 16; ++i) af[i] = trfrag(L + L_WO + ((w >> 1) * 256 + (i >> 3) * 128 + (w & 1) * 64 + ((i >> 2) & 1) * 32 + 8 * g + tq_) * 128 + (((i & 3) ^ f2) * 32) + tp * 8, 4 * 128);
    if (EARLY_FRAGS && w >= 2) WG_BARRIER();
    const int kv_ = sk * 4 + (w >> 1);
    v4u of[4], ls;
#pragma unroll 1
    for (int s = 0; s < SPIN_CAP; ++s) {
      ls = __builtin_amdgcn_raw_buffer_load_b128(rws, WS_LSE + (kv_ * 16 + n) * 16, 0, AUX_SC1);
#pragma unroll
      for (int i = 0; i < 4; ++i) of[i] = __builtin_amdgcn_raw_buffer_load_b128(rws, WS_O + kv_ * 8192 + (w & 1) * 4096 + n * 64 + g * 16 + i * 1024, 0, AUX_SC1);
      const v4u xx = (of[0] ^ of[1]) ^ (of[2] ^ of[3]);
      const unsigned ck = xrow_xor((xx.x ^ xx.y) ^ (xx.z ^ xx.w)) ^ ls.x ^ __builtin_rotateleft32(ls.y, 13) ^ thint ^ ((w & 1) ? ls.w : ls.z);
      if (ck == 0u) break;
      __builtin_amdgcn_s_sleep(1);
      CBAR();
    }
    if (sn == 0) {
      unsigned tk = 0;
      if (lane == 0) tk = __hip_atomic_fetch_add((LAS unsigned*)(FLG + F_GOT), 1u, __ATOMIC_RELAXED, __HIP_MEMORY_SCOPE_WORKGROUP);
      if (__builtin_amdgcn_readfirstlane(tk) == 7u) {
        __builtin_amdgcn_raw_buffer_store_b32(thint, rws, WS_HINT + lane * 128 + sk * 4, 0, AUX_SC1);
        __builtin_amdgcn_raw_buffer_store_b32(thint, rws, lane < 8 ? WS_HINT + 8192 + lane * 128 + sk * 4 : OOB, 0, AUX_SC1);
      }
    }
    f32x4 d[8];
#pragma unroll
    for (int t = 0; t < 8; ++t) d[t] = f32x4{0.f, 0.f, 0.f, 0.f};
#pragma unroll
    for (int ks = 0; ks < 2; ++ks) {
      const v4u b0 = {of[2 * ks].x, of[2 * ks].z, of[2 * ks + 1].x, of[2 * ks + 1].z}, b1 = {of[2 * ks].y, of[2 * ks].w, of[2 * ks + 1].y, of[2 * ks + 1].w};
#pragma unroll
      for (int t = 0; t < 4; ++t) { d[t] = mfma32(af[ks * 4 + t], b0, d[t]); d[4 + t] = mfma32(af[8 + ks * 4 + t], b1, d[4 + t]); }
    }
    const float e0 = U2F(ls.x), e1 = U2F(ls.y);
    const float w0 = __builtin_amdgcn_rcpf(1.f + __builtin_amdgcn_exp2f(dppf<QP_XOR1>(e0) - e0)), w1 = __builtin_amdgcn_rcpf(1.f + __builtin_amdgcn_exp2f(dppf<QP_XOR1>(e1) - e1));
    {
      const bool od = n & 1;
      f32x4 r[4];
#pragma unroll
      for (int t = 0; t < 4; ++t) r[t] = w0 * d[t] + w1 * d[4 + t];
      f32x4 k0 = od ? r[2] : r[0], k1 = od ? r[3] : r[1], s0 = od ? r[0] : r[2], s1 = od ? r[1] : r[3];
      k0.x += dppf<QP_XOR1>(s0.x); k0.y += dppf<QP_XOR1>(s0.y); k0.z += dppf<QP_XOR1>(s0.z); k0.w += dppf<QP_XOR1>(s0.w);
      k1.x += dppf<QP_XOR1>(s1.x); k1.y += dppf<QP_XOR1>(s1.y); k1.z += dppf<QP_XOR1>(s1.z); k1.w += dppf<QP_XOR1>(s1.w);
      LAS unsigned char* dst = L + L_RED1 + w * 2048 + (n >> 1) * 256 + (od ? 128 : 0) + g * 16;
      *(LAS f32x4*)dst = k0; *(LAS f32x4*)(dst + 64) = k1; }
    WG_BARRIER();
    {
      f32x4 t = *(LAS f32x4*)(L + L_RED1 + g * 4096 + w * 256 + n * 16) + *(LAS f32x4*)(L + L_RED1 + g * 4096 + 2048 + w * 256 + n * 16);
      t.x = xrow_sum(t.x); t.y = xrow_sum(t.y); t.z = xrow_sum(t.z); t.w = xrow_sum(t.w);
      __builtin_amdgcn_raw_buffer_store_b128(tagged(t, tg), rws, lane < 16 ? WS_H + ((w * 4 + sk) * 576 + sn * 64 + n * 4) * 4 : OOB, 0, AUX_SC1);
    }
  } else if (isF) {
    const bool dwave = w >= 2 && w < 7;
    const int dbk = w - 2;
    if (EARLY_FRAGS && w >= 2) WEIGHTS_LANDED(); else WG_BARRIER();
    v2u xr[3];
    { const rsrc_t rx = mkrs(x, 8 * 576 * 2);
#pragma unroll
      for (int i = 0; i < 3; ++i) xr[i] = __builtin_amdgcn_raw_buffer_load_b64(rx, 8 * (live ? lane : 47) + 384 * i, w * 1152, 0); }
    v4u df[16];
#define DOWN_LOADS() do { const rsrc_t rd = mkrs(wdn, 4608 * 576 * 2); \
      const unsigned vo_ = (dbk < 4 || n < 8) ? (unsigned)(fu * 64 + 8 * g) * 1152u + dbk * 256 + n * 16 : OOB; \
      _Pragma("unroll") for (int i = 0; i < 16; ++i) df[i] = __builtin_amdgcn_raw_buffer_load_b128(rd, vo_, ((i >> 3) * 32 + (i & 7)) * 1152, AUX_NT); SB(); } while (0)
    if (!EARLY_FRAGS && dwave) DOWN_LOADS();
    v4u gf[18];
    { LAS unsigned char* ga = L + ((tp & 1) ? L_UP : L_GATE) + (8 * g + tq_) * 128 + ((w ^ (f2 | ((tp & 1) << 2))) * 16) + (tp >> 1) * 8;
#pragma unroll
      for (int ks = 0; ks < 18; ++ks) gf[ks] = trfrag(ga + ks * 32 * 128, 4 * 128); }
#pragma unroll
    for (int ks = 0; ks < 18; ++ks) asm volatile("" : "+v"(gf[ks]));
    int hoff[3];
#pragma unroll
    for (int i = 0; i < 3; ++i) { const int c = 24 * i + (lane >> 1); hoff[i] = L_XN + w * HN_STR + (c & 3) * 288 + (c >> 2) * 16 + (lane & 1) * 8; asm volatile("" : "+v"(hoff[i])); }
    if (EARLY_FRAGS && w >= 2) { asm volatile("" : "+v"(xr[0]), "+v"(xr[1]), "+v"(xr[2])); WG_BARRIER(); if (dwave) DOWN_LOADS(); }
    if (dwave) {
#pragma unroll
      for (int ks = 0; ks < 2; ++ks) { v4u t_[8];
#pragma unroll
        for (int jj = 0; jj < 8; ++jj)
#pragma unroll
          for (int mm = 0; mm < 4; ++mm) t_[jj][mm] = __builtin_amdgcn_perm(df[ks * 8 + 2 * mm + 1][jj >> 1], df[ks * 8 + 2 * mm][jj >> 1], (jj & 1) ? 0x07060302u : 0x05040100u);
#pragma unroll
        for (int jj = 0; jj < 8; ++jj) { df[ks * 8 + jj] = t_[jj]; asm volatile("" : "+v"(df[ks * 8 + jj])); } }
    }
    asm volatile("" : "+v"(xr[0]), "+v"(xr[1]), "+v"(xr[2]));
    if (w == 7) {
#pragma unroll 1
      for (int s = 0; s < SPIN_CAP; ++s) {
        if (__builtin_amdgcn_raw_buffer_load_b32(rws, WS_HINT + fu * 128 + (lane & 3) * 4, 0, AUX_SC1) == thint) break;
        __builtin_amdgcn_s_sleep(2);
        CBAR();
      }
      __builtin_amdgcn_s_sleep(HP_DELAY);
    }
    WG_BARRIER();
    v4u hp[12];
    unsigned pend = 0u;
#define H_LOAD(q) _Pragma("unroll") for (int i = 3 * (q); i < 3 * (q) + 3; ++i) hp[i] = __builtin_amdgcn_raw_buffer_load_b128(rws, WS_H + (w * 4 * 576 + 4 * (live ? lane : 47)) * 4, i * 768, AUX_SC1)
#define H_BAD(q) ({ unsigned bad = 0; _Pragma("unroll") for (int i = 3 * (q); i < 3 * (q) + 3; ++i) untag(hp[i], tg, bad); __any((bad & 0x3ffu) != 0u); })
#pragma unroll
    for (int q = 0; q < 4; ++q) H_LOAD(q);
    SB();
#pragma unroll
    for (int q = 0; q < 4; ++q) pend |= H_BAD(q) ? 1u << q : 0u;
#pragma unroll 1
    for (int s = 0; pend != 0u && s < SPIN_CAP; ++s) {
      CBAR();
#pragma unroll
      for (int q = 0; q < 4; ++q) if ((pend >> q) & 1u) { H_LOAD(q); }
      SB();
#pragma unroll
      for (int q = 0; q < 4; ++q) if ((pend >> q) & 1u) { if (!H_BAD(q)) pend &= ~(1u << q); }
    }
    { float v[12];
#pragma unroll
      for (int i = 0; i < 3; ++i) {
        const f32x4 a = (__builtin_bit_cast(f32x4, hp[i]) + __builtin_bit_cast(f32x4, hp[3 + i])) + (__builtin_bit_cast(f32x4, hp[6 + i]) + __builtin_bit_cast(f32x4, hp[9 + i]));
        const unsigned r0 = cvtpk(a.x, a.y), r1 = cvtpk(a.z, a.w);
        const unsigned h0 = cvtpk(bflo(xr[i].x) + bflo(r0), bfhi(xr[i].x) + bfhi(r0)), h1 = cvtpk(bflo(xr[i].y) + bflo(r1), bfhi(xr[i].y) + bfhi(r1));
        v[4 * i] = bflo(h0); v[4 * i + 1] = bfhi(h0); v[4 * i + 2] = bflo(h1); v[4 * i + 3] = bfhi(h1); }
      v2u o3[3]; ln_row(v, live, o3);
      if (live) {
#pragma unroll
        for (int i = 0; i < 3; ++i) *(LAS v2u*)(L + hoff[i]) = o3[i]; } }
    WG_BARRIER();
    {
      v4u hb[9];
#pragma unroll
      for (int ks = 0; ks < 9; ++ks) hb[ks] = *(LAS v4u*)(L + L_XN + (n & 7) * HN_STR + g * 288 + (n >> 3) * 144 + ks * 16);
      SB();
      f32x4 d = {0.f, 0.f, 0.f, 0.f}, d2 = {0.f, 0.f, 0.f, 0.f};
#pragma unroll
      for (int ks = 0; ks < 9; ++ks) { d = mfma32(gf[ks], hb[ks], d); d2 = mfma32(gf[9 + ks], hb[ks], d2); }
      d.x += dppf<ROR8>(d2.x); d.y += dppf<ROR8>(d2.y); d.z += dppf<ROR8>(d2.z); d.w += dppf<ROR8>(d2.w);
      auto s0 = __builtin_amdgcn_permlane16_swap(F2U(d.x), F2U(d.z), false, false);
      auto s1 = __builtin_amdgcn_permlane16_swap(F2U(d.y), F2U(d.w), false, false);
      float av[2];
#pragma unroll
      for (int e = 0; e < 2; ++e) {
        const float gg = rbf(U2F(e ? s1[0] : s0[0])), uu = rbf(U2F(e ? s1[1] : s0[1]));
        const float z2 = gg * fmaf(gg * gg, 0.044715f, 1.f) * (2.f * 0.7978845608028654f * LOG2E);
        av[e] = rbf(gg * __builtin_amdgcn_rcpf(1.f + __builtin_amdgcn_exp2f(-z2))) * uu; }
      unsigned pk = cvtpk(av[0], av[1]);
      asm volatile("" : "+v"(pk));
      if (n < 8) *(LAS unsigned*)(L + L_ACT + n * ACT_STR + (8 * w + 2 * g) * 2) = pk; }
    WG_BARRIER();
    if (dwave) {
      const v4u b0 = *(LAS v4u*)(L + L_ACT + (n & 7) * ACT_STR + g * 16), b1 = *(LAS v4u*)(L + L_ACT + (n & 7) * ACT_STR + 64 + g * 16);
      f32x4 d[8];
#pragma unroll
      for (int jj = 0; jj < 8; ++jj) d[jj] = mfma32(df[jj], b0, f32x4{0.f, 0.f, 0.f, 0.f});
#pragma unroll
      for (int jj = 0; jj < 8; ++jj) d[jj] = mfma32(df[8 + jj], b1, d[jj]);
      const bool hq = n >= 8;
      const unsigned po = (dbk < 4 || g < 2) ? WS_P + ((32 * dbk + 8 * g + (n >> 3)) * 72 + fu) * 128 + (n & 7) * 16 : OOB;
#pragma unroll
      for (int e = 0; e < 4; ++e)
        __builtin_amdgcn_raw_buffer_store_b128(tagged(f32x4{hq ? d[4][e] : d[0][e], hq ? d[5][e] : d[1][e], hq ? d[6][e] : d[2][e], hq ? d[7][e] : d[3][e]}, tg), rws, po, 2 * e * 72 * 128, AUX_SC1);
    }
  } else {
    const int unit = rp < 108 ? rp : 108 + (rp - 108) * 2 + half;
    if (unit < 144) {
      const int ob_ = lane & 7, oc = unit * 4;
      WG_BARRIER();
      unsigned hh0 = 0, hh1 = 0;
      if (w == 7) {
        const v2u xo = __builtin_amdgcn_raw_buffer_load_b64(mkrs(x, 8 * 576 * 2), (ob_ * 576 + oc) * 2, 0, 0);
        v4u hv;
#pragma unroll 1
        for (int s = 0; s < SPIN_CAP; ++s) {
          hv = __builtin_amdgcn_raw_buffer_load_b128(rws, WS_H + ((ob_ * 4 + ((lane >> 3) & 3)) * 576 + oc) * 4, 0, AUX_SC1);
          unsigned bad = 0; untag(hv, tg, bad);
          if ((bad & 0x3ffu) == 0u) break;
          __builtin_amdgcn_s_sleep(16);
          CBAR();
        }
        f32x4 a = __builtin_bit_cast(f32x4, hv);
        a.x += dppf<ROR8>(a.x); a.y += dppf<ROR8>(a.y); a.z += dppf<ROR8>(a.z); a.w += dppf<ROR8>(a.w);
        a.x = row2_sum(a.x); a.y = row2_sum(a.y); a.z = row2_sum(a.z); a.w = row2_sum(a.w);
        const unsigned r0 = cvtpk(a.x, a.y), r1 = cvtpk(a.z, a.w);
        hh0 = cvtpk(bflo(xo.x) + bflo(r0), bfhi(xo.x) + bfhi(r0)); hh1 = cvtpk(bflo(xo.y) + bflo(r1), bfhi(xo.y) + bfhi(r1));
      }
      WG_BARRIER();
      v4u p0, p1;
#pragma unroll 1
      for (int s = 0; s < SPIN_CAP; ++s) {
        p0 = __builtin_amdgcn_raw_buffer_load_b128(rws, WS_P + unit * 9216 + w * 1024 + lane * 16, 0, AUX_SC1);
        p1 = __builtin_amdgcn_raw_buffer_load_b128(rws, WS_P + unit * 9216 + 8192 + w * 128 + (lane & 7) * 16, 0, AUX_SC1);
        unsigned bad = 0; untag(p0, tg, bad); untag(p1, tg, bad);
        if ((bad & 0x3ffu) == 0u) break;
        __builtin_amdgcn_s_sleep(1);
        CBAR();
      }
      f32x4 t = __builtin_bit_cast(f32x4, p0);
      t.x += dppf<ROR8>(t.x); t.y += dppf<ROR8>(t.y); t.z += dppf<ROR8>(t.z); t.w += dppf<ROR8>(t.w);
      t.x = xrow_sum(t.x); t.y = xrow_sum(t.y); t.z = xrow_sum(t.z); t.w = xrow_sum(t.w);
      if (lane < 8) *(LAS f32x4*)(L + L_RED + w * 128 + lane * 16) = t + __builtin_bit_cast(f32x4, p1);
      WG_BARRIER();
      if (w == 7) {
        f32x4 f = *(LAS f32x4*)(L + L_RED + lane * 16);
        f.x += dppf<ROR8>(f.x); f.y += dppf<ROR8>(f.y); f.z += dppf<ROR8>(f.z); f.w += dppf<ROR8>(f.w);
        f.x = xrow_sum(f.x); f.y = xrow_sum(f.y); f.z = xrow_sum(f.z); f.w = xrow_sum(f.w);
        const unsigned f0 = cvtpk(f.x, f.y), f1 = cvtpk(f.z, f.w);
        __builtin_amdgcn_raw_buffer_store_b64(v2u{cvtpk(bflo(hh0) + bflo(f0), bfhi(hh0) + bfhi(f0)), cvtpk(bflo(hh1) + bflo(f1), bfhi(hh1) + bfhi(f1))},
            mkrs(out, 8 * 576 * 2), lane < 8 ? (ob_ * 576 + oc) * 2 : OOB, 0, 0);
        __builtin_amdgcn_raw_buffer_store_b64(v2u{(unsigned)tag, thint}, rws, (unit == 0 && lane == 0) ? WS_EPOCH : OOB, 0, AUX_SC1);
      }
    }
  }
  asm volatile(".Lfb_code_end:");
}

extern "C" void kernel_launch(void* const* d_in, const int* in_sizes, int n_in, void* d_out, int out_size, void* d_ws, size_t ws_size, hipStream_t stream) {
  fused_block<<<256, 512, 0, stream>>>((const u16*)d_in[0], (const u16*)d_in[1], (const u16*)d_in[2], (const u16*)d_in[3], (const u16*)d_in[4],
      (const u16*)d_in[6], (const u16*)d_in[7], (const u16*)d_in[8], (u16*)d_out, (unsigned char*)d_ws);
}
```

```cpp
#include <hip/hip_runtime.h>

typedef unsigned short u16;
typedef unsigned long long u64;
typedef unsigned v4u __attribute__((ext_vector_type(4)));
typedef unsigned v2u __attribute__((ext_vector_type(2)));
typedef float f32x4 __attribute__((ext_vector_type(4)));
typedef float f32x2 __attribute__((ext_vector_type(2)));
typedef short bf16x8 __attribute__((ext_vector_type(8)));
typedef short s16x4 __attribute__((ext_vector_type(4)));
typedef __bf16 bf16x2_t __attribute__((ext_vector_type(2)));
typedef __amdgpu_buffer_rsrc_t rsrc_t;
#define LAS __attribute__((address_space(3)))
#define STR2(x) #x
#define STR(x) STR2(x)

#define TAU 54
#define T_TARGET 3700
#define LEAD_F 60
#define WINDOW 9
#define TH1 50
#define W1 10
#define TH2 120
#define W2 11
#define TH3 250
#define W3 13
#define DMA_INFL 12
#define QPOLL_DELAY 40
#define HP_DELAY 5
#define PK0 0
#define PK1 1
#define PK2 2
#define PT0 4
#define PT1 5
#define PW 3
#define PH 7
#define PI 6
#define QM 63u

constexpr int WS_EPOCH = 0;
constexpr int WS_CHK = 8192;
constexpr int WS_H = 16384;
constexpr int WS_HINT = 98304;
constexpr int WS_QG = 131072;
constexpr int WS_O = WS_QG + 8 * 4 * 4096 * 4;
constexpr int WS_P = WS_O + 16 * 8192;
constexpr int WS_X = WS_P + 144 * 72 * 128;
constexpr int WS_END = WS_X + 256 * 128;
constexpr int L_GATE = 0, L_UP = 73728, L_WO = 0, L_TILE = 4096, L_WQ = 129024;
constexpr int L_XN = 147456, XN_STR = 1168, HN_STR = 1216;
constexpr int L_SC = L_XN, SC_STR = 17 * 256;
constexpr int L_ACT = L_XN + 8 * HN_STR, ACT_STR = 144;
constexpr int L_RED = L_ACT + 10 * ACT_STR;
constexpr int L_Q = L_RED + 1024;
constexpr int L_FLG = L_Q + 512;
constexpr int L_SINK = L_FLG + 256;
constexpr int L_PAD = L_SINK + 256;
constexpr int L_TOTAL = L_PAD + 512;
static_assert(L_TOTAL <= 163840 && L_TOTAL > 81920, "one workgroup per CU");
constexpr int F_QREADY = 0, F_QDONE = 1, F_PDONE = 2, F_TICK = 3, F_KDONE = 10, F_LSE = 11, F_LSEV = 12, F_GOT = 4, F_XN = 5, F_FULL = 6, F_TAG = 8;

constexpr u64 TAG_STEP = 0x9E3779ull << 40 | 0x16Bull << 30 | 0x2D5ull << 20 | 0x0F3ull << 10 | 0x1B5ull;
#define TAG_FIELDS(t) v4u{(unsigned)(t) & 0x3ffu, ((unsigned)(t) >> 10) & 0x3ffu, ((unsigned)(t) >> 20) & 0x3ffu, (unsigned)((t) >> 30) & 0x3ffu}
constexpr int AUX_NT = 2, AUX_SC1 = 16;
constexpr unsigned OOB = 0x7FFFFFF0u;
constexpr int SPIN_CAP = 1 << 17, LDS_SPIN_CAP = 1 << 22;
constexpr float LOG2E = 1.4426950408889634f;

#define SB() __builtin_amdgcn_sched_barrier(0)
#define CBAR() asm volatile("" ::: "memory")
#define WG_BARRIER() asm volatile("s_waitcnt lgkmcnt(0)\n\ts_barrier" ::: "memory")
#define U2F __uint_as_float
#define F2U __float_as_uint

__device__ __forceinline__ f32x4 mfma32(v4u a, v4u b, f32x4 c) {
  return __builtin_amdgcn_mfma_f32_16x16x32_bf16(__builtin_bit_cast(bf16x8, a), __builtin_bit_cast(bf16x8, b), c, 0, 0, 0);
}
__device__ __forceinline__ unsigned cvtpk(float lo, float hi) { f32x2 v = {lo, hi}; return __builtin_bit_cast(unsigned, __builtin_convertvector(v, bf16x2_t)); }
__device__ __forceinline__ float rbf(float x) { return U2F(cvtpk(x, x) & 0xffff0000u); }
__device__ __forceinline__ float bflo(unsigned x) { return U2F(x << 16); }
__device__ __forceinline__ float bfhi(unsigned x) { return U2F(x & 0xffff0000u); }
template <int CTRL> __device__ __forceinline__ unsigned dppu(unsigned x) { return (unsigned)__builtin_amdgcn_mov_dpp((int)x, CTRL, 0xf, 0xf, true); }
template <int CTRL> __device__ __forceinline__ float dppf(float x) { return U2F(dppu<CTRL>(F2U(x))); }
constexpr int QP_XOR1 = 0xB1, QP_XOR2 = 0x4E, ROR4 = 0x124, ROR8 = 0x128, HALF_MIRROR = 0x141;
constexpr int QP_0011 = 0x50, QP_2233 = 0xFA, QP_0000 = 0x00, QP_1111 = 0x55, QP_ID = 0xE4;
__device__ __forceinline__ float xrow_max(float x) {
  auto s = __builtin_amdgcn_permlane16_swap(F2U(x), F2U(x), false, false);
  x = fmaxf(U2F(s[0]), U2F(s[1]));
  auto t = __builtin_amdgcn_permlane32_swap(F2U(x), F2U(x), false, false);
  return fmaxf(U2F(t[0]), U2F(t[1]));
}
__device__ __forceinline__ float row2_sum(float x) {
  auto s = __builtin_amdgcn_permlane16_swap(F2U(x), F2U(x), false, false);
  return U2F(s[0]) + U2F(s[1]);
}
__device__ __forceinline__ float xrow_sum(float x) {
  x = row2_sum(x);
  auto t = __builtin_amdgcn_permlane32_swap(F2U(x), F2U(x), false, false);
  return U2F(t[0]) + U2F(t[1]);
}
__device__ __forceinline__ unsigned xrow_xor(unsigned x) {
  auto s = __builtin_amdgcn_permlane16_swap(x, x, false, false);
  x = s[0] ^ s[1];
  auto t = __builtin_amdgcn_permlane32_swap(x, x, false, false);
  return t[0] ^ t[1];
}
__device__ __forceinline__ float wave_sum(float x) {
  x += dppf<QP_XOR1>(x); x += dppf<QP_XOR2>(x); x += dppf<ROR4>(x); x += dppf<ROR8>(x);
  return xrow_sum(x);
}
template <int SRC, int DST> __device__ __forceinline__ unsigned bank_mov(unsigned old, unsigned x) {
  constexpr int sh = ((DST - SRC) & 3) * 4;
  return (unsigned)__builtin_amdgcn_update_dpp((int)old, (int)x, sh == 0 ? QP_ID : (0x120 + sh), 0xf, 1 << DST, false);
}
__device__ __forceinline__ rsrc_t mkrs(const void* p, unsigned bytes) { return __builtin_amdgcn_make_buffer_rsrc((void*)p, 0, bytes, 0x00020000); }
__device__ __forceinline__ v4u trfrag(LAS unsigned char* p, int rows4) {
  const v2u a = __builtin_bit_cast(v2u, __builtin_amdgcn_ds_read_tr16_b64_v4i16((LAS s16x4*)p));
  const v2u b = __builtin_bit_cast(v2u, __builtin_amdgcn_ds_read_tr16_b64_v4i16((LAS s16x4*)(p + rows4)));
  return v4u{a.x, a.y, b.x, b.y};
}
__device__ __forceinline__ v4u tagged(f32x4 v, v4u t, unsigned keep) { return (__builtin_bit_cast(v4u, v) & keep) | t; }
__device__ __forceinline__ void untag(v4u& v, v4u t, unsigned& bad) { v ^= t; bad |= (v.x | v.y) | (v.z | v.w); }
__device__ __forceinline__ unsigned opad(unsigned pos, unsigned t) { unsigned z = (pos + 1u) * 0x9E3779B1u ^ t; z ^= z >> 15; z *= 0x85EBCA77u; z ^= z >> 13; z *= 0xC2B2AE3Du; return z ^ (z >> 16); }
__device__ __forceinline__ void lds_wait_ge(volatile LAS unsigned* p, unsigned v) {
  for (int s = 0; *p < v && s < LDS_SPIN_CAP; ++s) __builtin_amdgcn_s_sleep(1);
  CBAR();
}
__device__ __forceinline__ void lds_spin_ge(volatile LAS unsigned* p, unsigned v) {
  for (int s = 0; *p < v && s < LDS_SPIN_CAP; ++s) { }
  CBAR();
}
__device__ __forceinline__ void ln_row(const float (&v)[12], bool live, v2u (&out)[3]) {
  const float ref = U2F(__builtin_amdgcn_readfirstlane(F2U(v[0])));
  float s1 = 0.f, s2 = 0.f, t1 = 0.f, t2 = 0.f;
#pragma unroll
  for (int i = 0; i < 12; i += 2) { const float d = v[i] - ref, e = v[i + 1] - ref; s1 += d; s2 = fmaf(d, d, s2); t1 += e; t2 = fmaf(e, e, t2); }
  s1 = wave_sum(live ? s1 + t1 : 0.f); s2 = wave_sum(live ? s2 + t2 : 0.f);
  const float md = s1 * (1.f / 576.f);
  const float var = fmaxf(fmaf(-md, md, s2 * (1.f / 576.f)), 0.f);
  const float m = rbf(ref + md);
  const float inv = __builtin_amdgcn_rcpf(rbf(__builtin_amdgcn_sqrtf(rbf(rbf(var) + 1e-5f))));
#pragma unroll
  for (int i = 0; i < 3; ++i) {
    const unsigned a = cvtpk(v[4 * i] - m, v[4 * i + 1] - m), c = cvtpk(v[4 * i + 2] - m, v[4 * i + 3] - m);
    out[i] = v2u{cvtpk(bflo(a) * inv, bfhi(a) * inv), cvtpk(bflo(c) * inv, bfhi(c) * inv)};
  }
}

__global__ void __launch_bounds__(512, 2) fused_block(const u16* __restrict__ x, const u16* __restrict__ Kc, const u16* __restrict__ Vc, const u16* __restrict__ wq,
    const u16* __restrict__ wo, const u16* __restrict__ wgt, const u16* __restrict__ wup, const u16* __restrict__ wdn, u16* out, unsigned char* ws) {
  __shared__ __attribute__((aligned(16))) unsigned char smem_[L_TOTAL];
  LAS unsigned char* L = (LAS unsigned char*)smem_;
  volatile LAS unsigned* FLG = (volatile LAS unsigned*)(L + L_FLG);
  const int tid = threadIdx.x, lane = tid & 63, w = __builtin_amdgcn_readfirstlane(tid >> 6);
#define MKDESC(p, bytes) v4u{(unsigned)(u64)(p), (unsigned)((u64)(p) >> 32) & 0xffffu, (unsigned)(bytes), 0x00020000u}
  const unsigned lbase = (unsigned)(size_t)L;
#define DMA_ASM(desc, ldsa, voff, soff, imm) asm volatile("s_mov_b32 m0, %0\n\ts_nop 0\n\tbuffer_load_dwordx4 %1, %2, %3 offen offset:%4 nt lds" \
    :: "s"((ldsa) - (imm)), "v"(voff), "s"(desc), "s"(soff), "i"(imm) : "memory")
#define DMA_PACE() asm volatile("s_waitcnt vmcnt(" STR(DMA_INFL) ")" ::: "memory")
#define LDS_INC(f) do { if (lane == 0) __hip_atomic_fetch_add((LAS unsigned*)(FLG + (f)), 1u, __ATOMIC_RELAXED, __HIP_MEMORY_SCOPE_WORKGROUP); } while (0)
  const bool live = lane < 48, two = (w == 2 || w == 7);
  const unsigned sw2 = (unsigned)((((lane & 7) ^ (((lane >> 4) & 1) << 1)) * 16) ^ (w << 6));
  v2u ep, xpk[3], xp2[3];
  if (w < 2) {
    {
      u64 pc; unsigned len;
      asm volatile("s_getpc_b64 %0\n.Lfb_code_begin:\n\ts_mov_b32 %1, .Lfb_code_end-.Lfb_code_begin" : "=s"(pc), "=s"(len));
      const v4u dc = MKDESC(pc, len);
      asm volatile("s_mov_b32 m0, %0\n\ts_nop 0\n\tbuffer_load_dword %1, %2, 0 offen lds\n\tbuffer_load_dword %3, %2, 0 offen lds"
        :: "s"(lbase + L_SINK), "v"(lane * 128 + w * 8192), "s"(dc), "v"(lane * 128 + w * 8192 + 16384) : "memory"); }
    const v4u dq = MKDESC(wq, 576 * 4096 * 2);
    const unsigned vo = (unsigned)((blockIdx.x & 3) * 144 + w * 8 + (lane >> 3)) * 8192u + (blockIdx.x >> 2) * 128 + sw2;
#pragma unroll
    for (int i = 0; i < 9; ++i) DMA_ASM(dq, lbase + L_WQ + w * 1024 + i * 2048, vo, i * 16 * 8192, 0);
    asm volatile("" : "=v"(ep), "=v"(xpk[0]), "=v"(xpk[1]), "=v"(xpk[2]), "=v"(xp2[0]), "=v"(xp2[1]), "=v"(xp2[2]));
  } else {
    const unsigned xo = (unsigned)(8 * (live ? lane : 47)), xo1 = xo + w * 1152, xo2 = xo + (w & 1) * 1152, z = 0;
    asm volatile("global_load_dwordx2 %0, %3, %4\n\tglobal_load_dwordx2 %1, %3, %4 offset:384\n\tglobal_load_dwordx2 %2, %3, %4 offset:768"
      : "=&v"(xpk[0]), "=&v"(xpk[1]), "=&v"(xpk[2]) : "v"(xo1), "s"(x) : "memory");
    asm volatile("global_load_dwordx2 %0, %1, %2 sc1" : "=&v"(ep) : "v"(z), "s"(ws) : "memory");
    if (two) asm volatile("global_load_dwordx2 %0, %3, %4\n\tglobal_load_dwordx2 %1, %3, %4 offset:384\n\tglobal_load_dwordx2 %2, %3, %4 offset:768"
      : "=&v"(xp2[0]), "=&v"(xp2[1]), "=&v"(xp2[2]) : "v"(xo2), "s"(x) : "memory");
    else asm volatile("" : "=v"(xp2[0]), "=v"(xp2[1]), "=v"(xp2[2]));
    if (w == 2) FLG[lane] = 0u;
  }
  WG_BARRIER();
  int wg = blockIdx.x;
  asm volatile("" : "+s"(wg));
  const int pair = wg >> 1, half = wg & 1, b = pair >> 4, kvh = pair & 15;
  const int rp = (pair & 7) * 16 + (pair >> 3);
  const bool holder = half == ((pair >> 3) & 1);
  const bool isF = holder && rp < 72, isO = holder && rp >= 72 && rp < 108;
  const int fu = rp;
  const int sk = (rp - 72) & 3, sn = (rp - 72) >> 2;
  const int cb = wg >> 2, kq = wg & 3;
  const int n = lane & 15, g = lane >> 4, tq_ = (lane & 15) >> 2, tp = lane & 3;
  const int f2 = (tq_ >> 1) | ((g & 1) << 1);
  const rsrc_t rws = mkrs(ws, WS_END);
  const int ND = rp < 108 ? (holder ? 15 : 17) : 16;
  const unsigned tsched = (unsigned)__builtin_amdgcn_s_memrealtime() + (unsigned)(T_TARGET - (isF ? LEAD_F : 0) - 4 * ND * TAU);
  const unsigned pbase = (unsigned)pair << 20;
  const int hflip = (int)((0x6B5A93C1D2E4F078ull >> (pair >> 2)) & 1);
#define UOFF(u, ww) (((u) + 16 < ND) ? pbase + (((unsigned)(ww) << PW) | ((unsigned)(half ^ hflip ^ 1) << PH)) * 256u \
                                     : pbase + (unsigned)((u) + 16 - ND) * 65536u + (((unsigned)(ww) << PW) | ((unsigned)(half ^ hflip) << PH)) * 256u)
#define K8(v) ((((v) & 1) << PK0) | ((((v) >> 1) & 1) << PK1) | ((((v) >> 2) & 1) << PK2))
#define TT(v) ((((v) & 1) << PT0) | ((((v) >> 1) & 1) << PT1))
  static_assert(PK0 < 4 && PK1 < 4 && PK2 < 4, "V instruction offsets are immediates");
  const int voffK = K8(n >> 1) * 256 + (2 * g + (n & 1)) * 16;
  const int voffV = TT(g) * 256 + n * 16;
  v4u rg[16];
#define KSO(i) ((TT(((i) >> 1) & 3) | (((i) >> 3) << PI)) * 256)
#define LDK(i, so) rg[i] = __builtin_amdgcn_raw_buffer_load_b128(rk, voffK + ((i) & 1) * 128, (so) + KSO(i), AUX_NT)
#define LDV(i, so) rg[i] = __builtin_amdgcn_raw_buffer_load_b128(rv, voffV + K8((i) & 7) * 256, (so) + (((i) >> 3) << PI) * 256, AUX_NT)
  const rsrc_t rk = mkrs(Kc, 128u << 20), rv = mkrs(Vc, 128u << 20);
  const int nrounds = (isF || isO) ? 0 : 4;

  if (w < 2) {
    if (nrounds == 0) {
#pragma unroll 1
      for (int run = 0; run < 2; ++run) {
        const int p0 = isF ? 72 * run : 64 * run, p1 = isF ? p0 + 72 : p0 + 64;
        const v4u dw = isF ? MKDESC(run ? wup : wgt, 576 * 4608 * 2) : MKDESC(wo, 4096 * 576 * 2);
        const unsigned vo = isF ? (unsigned)(lane >> 3) * 9216u + fu * 128 + (((lane & 7) ^ ((lane >> 4) & 1) ^ (w << 1) ^ (run << 2)) * 16)
                                : (unsigned)(sk * 1024 + (lane >> 3)) * 1152u + sn * 128 + sw2;
        const unsigned sstep = isF ? 16u * 9216u : 16u * 1152u;
        unsigned so = (isF ? 0u : (unsigned)p0 * 8u * 1152u) + (sstep >> 1) * w, la = lbase + (p0 + w) * 1024;
        if (run) lds_wait_ge(FLG + F_QDONE, 4u);
#pragma unroll 1
        for (int i = p0; i < p1; i += 8) {
#pragma unroll
          for (int j = 0; j < 4; ++j) { DMA_ASM(dw, la, vo, so, 0); DMA_PACE(); la += 2048; so += sstep; }
          if (i == 16) LDS_INC(F_XN);
        }
      }
    } else {
      const v4u dk = MKDESC(Kc, 128u << 20);
#pragma unroll 1
      for (int rd = 0; rd < 4; ++rd) {
        const int slot = 2 * rd + w;
        if (slot == 7) lds_wait_ge(FLG + F_QDONE, 4u);
        const unsigned so = UOFF(rd, w), la = lbase + L_TILE + slot * 16384;
#pragma unroll
        for (int i = 0; i < 16; ++i) { DMA_ASM(dk, la + i * 1024, voffK, so + KSO(i), (i & 1) * 128); DMA_PACE(); if (i == 11 && rd == 0) LDS_INC(F_XN); }
        FLG[F_FULL + w] = rd;
      }
    }
    SB();
#pragma unroll
    for (int i = 0; i < 16; ++i) { LDK(i, UOFF(nrounds, w)); SB(); DMA_PACE(); }
    FLG[F_FULL + w] = 4u;
  } else {
    if (two) asm volatile("s_waitcnt vmcnt(4)" ::: "memory"); else asm volatile("s_waitcnt vmcnt(1)" ::: "memory");
    asm volatile("" : "+v"(xpk[0]), "+v"(xpk[1]), "+v"(xpk[2]));
#define LN_TO_XN(src, row) do { float v[12]; \
      _Pragma("unroll") for (int i = 0; i < 3; ++i) { v[4 * i] = bflo(src[i].x); v[4 * i + 1] = bfhi(src[i].x); v[4 * i + 2] = bflo(src[i].y); v[4 * i + 3] = bfhi(src[i].y); } \
      v2u o3[3]; ln_row(v, live, o3); \
      if (live) { _Pragma("unroll") for (int i = 0; i < 3; ++i) *(LAS v2u*)(L + L_XN + (row) * XN_STR + (192 * i + 4 * lane) * 2) = o3[i]; } } while (0)
    LN_TO_XN(xpk, w);
    SB();
    asm volatile("s_waitcnt vmcnt(0)" : "+v"(ep), "+v"(xp2[0]), "+v"(xp2[1]), "+v"(xp2[2]) :: "memory");
    if (two) LN_TO_XN(xp2, w & 1);
    asm volatile("s_waitcnt lgkmcnt(0)" ::: "memory");
    LDS_INC(F_XN);
    const u64 tag = ((u64)(unsigned)__builtin_amdgcn_readfirstlane(ep.y) << 32 | (unsigned)__builtin_amdgcn_readfirstlane(ep.x)) + TAG_STEP;
    const unsigned tq32 = (unsigned)(tag >> 32);
    const v4u tq4 = TAG_FIELDS(tag) & QM;
    if (!two) {
      lds_spin_ge(FLG + F_XN, 8u);
      const int tq = w - 3;
      v4u a[5], bb[5];
#pragma unroll
      for (int ks = 0; ks < 5; ++ks) {
        const int gg = ks < 4 ? g : (g & 1);
        a[ks] = trfrag(L + L_WQ + (32 * ks + 8 * gg + tq_) * 128 + ((tq ^ f2) * 32) + tp * 8, 4 * 128);
        bb[ks] = *(LAS v4u*)(L + L_XN + (n & 7) * XN_STR + (kq * 144 + 32 * ks + 8 * gg) * 2);
      }
      SB();
      if (g >= 2) bb[4] = v4u{0, 0, 0, 0};
      f32x4 d = {0.f, 0.f, 0.f, 0.f};
#pragma unroll
      for (int ks = 0; ks < 5; ++ks) d = mfma32(a[ks], bb[ks], d);
      __builtin_amdgcn_raw_buffer_store_b128((__builtin_bit_cast(v4u, d) & ~QM) | tq4, rws, n < 8 ? WS_QG + ((n * 4 + kq) * 4096 + cb * 64 + tq * 16 + 4 * g) * 4 : OOB, 0, AUX_SC1);
      LDS_INC(F_QDONE);
    }
    if (w == 7) {
      FLG[F_TAG] = (unsigned)tag; FLG[F_TAG + 1] = tq32;
      *(LAS v2u*)(L + L_PAD + lane * 8) = v2u{opad(2 * lane, tq32), opad(2 * lane + 1, tq32)};
      CBAR();
      const int qo = WS_QG + (b * 4 * 4096 + kvh * 256 + 4 * lane) * 4;
      v4u a[4];
      __builtin_amdgcn_s_sleep(QPOLL_DELAY);
#pragma unroll 1
      for (int s = 0; s < SPIN_CAP; ++s) {
#pragma unroll
        for (int i = 0; i < 4; ++i) a[i] = __builtin_amdgcn_raw_buffer_load_b128(rws, qo, i * 16384, AUX_SC1);
        unsigned bad = 0;
#pragma unroll
        for (int i = 0; i < 4; ++i) untag(a[i], tq4, bad);
        bad |= dppu<QP_XOR1>(bad); bad |= dppu<QP_XOR2>(bad);
        if ((bad & QM) == 0u) break;
        __builtin_amdgcn_s_sleep(2);
        CBAR();
      }
#define QS(c) ((U2F(a[0].c | ((QM + 1u) >> 1)) + U2F(a[1].c | ((QM + 1u) >> 1))) + (U2F(a[2].c | ((QM + 1u) >> 1)) + U2F(a[3].c | ((QM + 1u) >> 1))))
      *(LAS v2u*)(L + L_Q + lane * 8) = v2u{cvtpk(QS(x), QS(y)), cvtpk(QS(z), QS(w))};
      asm volatile("s_waitcnt lgkmcnt(0)" ::: "memory");
      FLG[F_QREADY] = 1u;
    }
#pragma unroll
    for (int i = 0; i < 16; ++i) asm volatile("" : "=v"(rg[i]));
  }
  lds_wait_ge(FLG + F_QREADY, 1u);
  const u64 tag = (u64)(unsigned)__builtin_amdgcn_readfirstlane(FLG[F_TAG + 1]) << 32 | (unsigned)__builtin_amdgcn_readfirstlane(FLG[F_TAG]);
  const unsigned thint = (unsigned)(tag >> 32);
  const bool par = lane & 1;
#define SC(ww, u) (*(LAS unsigned*)(L + L_SC + (ww) * SC_STR + (u) * 256 + lane * 4))
#define QFRAGS() v4u qf[8]; \
    { const v4u qa0 = *(LAS v4u*)(L + L_Q + (((n >> 1) & 1) * 128 + 16 * g + 8 * (n & 1)) * 2), qa1 = *(LAS v4u*)(L + L_Q + (((n >> 1) & 1) * 128 + 64 + 16 * g + 8 * (n & 1)) * 2); \
      _Pragma("unroll") for (int i = 0; i < 8; ++i) qf[i] = ((n >> 2) == (i >> 1)) ? ((i & 1) ? qa1 : qa0) : v4u{0, 0, 0, 0}; }
#define AHEAD(z) const int ahead = (int)((unsigned)__builtin_amdgcn_s_memrealtime() - tsched - (unsigned)((2 * (z) + w) * TAU))
#define WSTEP(th, w_) "s_cmpk_ge_i32 %0, " STR(th) "\n\ts_cbranch_scc1 1f\n\ts_waitcnt vmcnt(" STR(w_) ")\n\t"
#define WINA() asm volatile(WSTEP(TH3, W3) WSTEP(TH2, W2) WSTEP(TH1, W1) WSTEP(0, WINDOW) "\n1:" :: "s"(ahead) : "memory", "scc")
#define KBODY(NEXT, ww, u) { \
      f32x4 d0 = {0.f, 0.f, 0.f, 0.f}, d1 = {0.f, 0.f, 0.f, 0.f}; \
      _Pragma("unroll") for (int i = 0; i < 16; ++i) { if (i < 8) d0 = mfma32(qf[i & 7], rg[i], d0); else d1 = mfma32(qf[i & 7], rg[i], d1); \
        if (NEXT == 1) { WINA(); LDK(i, so); } if (NEXT == 2) { WINA(); LDV(i, so); } SB(); } \
      const float s0 = (par ? d0.w : d0.x) + dppf<QP_XOR1>(par ? d0.y : d0.z), s1 = (par ? d1.w : d1.x) + dppf<QP_XOR1>(par ? d1.y : d1.z); \
      const unsigned r1 = cvtpk(s0, s1), r2 = cvtpk(bflo(r1) * 0.08838834764831845f, bfhi(r1) * 0.08838834764831845f); \
      SC(ww, u) = r2; \
      m = fmaxf(m, fmaxf(bflo(r2), bfhi(r2))); }
  if (w >= 2) {
    if (w < 7 && nrounds > 0) {
      lds_wait_ge(FLG + F_QDONE, 4u);
      QFRAGS();
      float m = 0.f;
#pragma unroll 1
      for (int t = w - 2; t < 8; t += 5) {
        lds_wait_ge(FLG + F_FULL + (t & 1), (unsigned)((t >> 1) + 1));
#pragma unroll
        for (int i = 0; i < 16; ++i) rg[i] = *(LAS v4u*)(L + L_TILE + t * 16384 + i * 1024 + lane * 16);
        const unsigned so = 0; const int ahead = 0; KBODY(0, t & 1, t >> 1)
        CBAR();
        LDS_INC(F_PDONE);
      }
    }
  } else {
    {
      lds_wait_ge(FLG + F_QDONE, 4u);
      float m = -INFINITY;
      { QFRAGS();
#pragma unroll 1
        for (int u = nrounds; u + 1 < ND; ++u) { const unsigned so = UOFF(u + 1, w); AHEAD(u + 1); KBODY(1, w, u) }
        { const unsigned so = UOFF(0, w); AHEAD(ND); KBODY(2, w, ND - 1) } }
      if (nrounds > 0) {
        lds_wait_ge(FLG + F_PDONE, 8u);
#pragma unroll
        for (int u = 0; u < 4; ++u) { const unsigned r = SC(w, u); m = fmaxf(m, fmaxf(bflo(r), bfhi(r))); }
      }
      CBAR();
      LDS_INC(F_KDONE);
      m = fmaxf(m, dppf<QP_XOR2>(m)); m = fmaxf(m, dppf<ROR4>(m)); m = fmaxf(m, dppf<ROR8>(m)); m = xrow_max(m);
      const unsigned jbmask = par ? 0xffff0000u : 0x0000ffffu;
      f32x4 o = {0.f, 0.f, 0.f, 0.f};
      v4u pa0 = {0, 0, 0, 0}, pa1 = {0, 0, 0, 0};
#define PV(i, pa, xs_, MORE) { pa.x = bank_mov<(((i) & 7) >> 1), 0>(pa.x, xs_); pa.y = bank_mov<(((i) & 7) >> 1), 1>(pa.y, xs_); pa.z = bank_mov<(((i) & 7) >> 1), 2>(pa.z, xs_); pa.w = bank_mov<(((i) & 7) >> 1), 3>(pa.w, xs_); \
        o = mfma32(pa, rg[i], o); if (MORE) { WINA(); LDV(i, so); } SB(); }
#define VBODY(MORE) { \
      const float p0 = __builtin_amdgcn_exp2f((bflo(sc) - m) * LOG2E), p1 = __builtin_amdgcn_exp2f((bfhi(sc) - m) * LOG2E); \
      const unsigned q0 = cvtpk(p0, p0), q1 = cvtpk(p1, p1); \
      const unsigned x0 = dppu<QP_0011>(q0) & jbmask, x1 = dppu<QP_2233>(q0) & jbmask, x2 = dppu<QP_0011>(q1) & jbmask, x3 = dppu<QP_2233>(q1) & jbmask; \
      if (MORE) sc = SC(w, u + 1); \
      SB(); \
      PV(0, pa0, x0, MORE) PV(1, pa1, x1, MORE) PV(2, pa0, x0, MORE) PV(3, pa1, x1, MORE) PV(4, pa0, x0, MORE) PV(5, pa1, x1, MORE) PV(6, pa0, x0, MORE) PV(7, pa1, x1, MORE) \
      PV(8, pa0, x2, MORE) PV(9, pa1, x3, MORE) PV(10, pa0, x2, MORE) PV(11, pa1, x3, MORE) PV(12, pa0, x2, MORE) PV(13, pa1, x3, MORE) PV(14, pa0, x2, MORE) PV(15, pa1, x3, MORE) }
      unsigned sc = SC(w, 0);
#pragma unroll 1
      for (int u = 0; u + 1 < ND; ++u) { const unsigned so = UOFF(u + 1, w); AHEAD(ND + u + 1); VBODY(1) }
      lds_wait_ge(FLG + F_LSE, 1u);
      const float cw = __builtin_amdgcn_exp2f((m - U2F(FLG[F_LSEV + (lane & 1)])) * LOG2E) * U2F(FLG[F_LSEV + 2 + (lane & 1)]);
      float c0 = dppf<QP_0000>(cw), c1 = dppf<QP_1111>(cw);
      v2u pad = *(LAS v2u*)(L + L_PAD + ((n >> 2) * 2 + (g >> 1)) * 64 + (n & 3) * 16 + (g & 1) * 8);
      int odst_ = WS_O + kvh * 8192 + ((n >> 2) * 2 + (g >> 1)) * 1024 + (b * 2 + half) * 64 + (n & 3) * 16 + (g & 1) * 8;
      unsigned cdst = (lane & 55) == 0 ? WS_CHK + (kvh * 16 + b * 2 + half) * 8 + (lane >> 1) : OOB;
      asm volatile("" : "+v"(pad), "+v"(odst_), "+v"(cdst), "+v"(c0), "+v"(c1));
      { const unsigned so = 0; const int ahead = 0, u = 0; VBODY(0) }
#define REC_O(r) (*(LAS f32x4*)(L + L_SC + (r) * SC_STR + lane * 16))
      const unsigned tk0 = FLG[F_TICK];
      CBAR();
      f32x4 ro = REC_O(1 - w);
      CBAR();
      o = f32x4{o.x * c0, o.y * c0, o.z * c1, o.w * c1};
      asm volatile("" : "+v"(o));
      bool pub = true;
      if (__builtin_amdgcn_readfirstlane(tk0) == 0u) {
        REC_O(w) = o;
        CBAR();
        unsigned tk = 0;
        if (lane == 0) tk = __hip_atomic_fetch_add((LAS unsigned*)(FLG + F_TICK), 1u, __ATOMIC_RELAXED, __HIP_MEMORY_SCOPE_WORKGROUP);
        CBAR();
        ro = REC_O(1 - w);
        CBAR();
        pub = __builtin_amdgcn_readfirstlane(tk) != 0u;
      }
      if (pub) {
        o += ro;
        const v2u pk = v2u{cvtpk(o.x, o.y), cvtpk(o.z, o.w)} ^ pad;
        __builtin_amdgcn_raw_buffer_store_b64(pk, rws, odst_, 0, AUX_SC1);
        unsigned ck = pk.x ^ pk.y;
        ck ^= dppu<QP_XOR1>(ck); ck ^= dppu<QP_XOR2>(ck); ck ^= dppu<HALF_MIRROR>(ck); ck = xrow_xor(ck);
        __builtin_amdgcn_raw_buffer_store_b32(ck ^ thint, rws, cdst, 0, AUX_SC1);
      }
    }
  }
  if (w == 2) {
    lds_wait_ge(FLG + F_KDONE, 2u);
    float mx = -INFINITY, ls = 0.f;
#pragma unroll 1
    for (int u = 0; u < ND; ++u) { const unsigned r0 = SC(0, u), r1 = SC(1, u); mx = fmaxf(fmaxf(mx, fmaxf(bflo(r0), bfhi(r0))), fmaxf(bflo(r1), bfhi(r1))); }
    mx = fmaxf(mx, dppf<QP_XOR2>(mx)); mx = fmaxf(mx, dppf<ROR4>(mx)); mx = fmaxf(mx, dppf<ROR8>(mx)); mx = xrow_max(mx);
#pragma unroll 1
    for (int u = 0; u < ND; ++u) { const unsigned r0 = SC(0, u), r1 = SC(1, u);
      ls += (__builtin_amdgcn_exp2f((bflo(r0) - mx) * LOG2E) + __builtin_amdgcn_exp2f((bfhi(r0) - mx) * LOG2E)) + (__builtin_amdgcn_exp2f((bflo(r1) - mx) * LOG2E) + __builtin_amdgcn_exp2f((bfhi(r1) - mx) * LOG2E)); }
    ls += dppf<QP_XOR2>(ls); ls += dppf<ROR4>(ls); ls += dppf<ROR8>(ls); ls = xrow_sum(ls);
    __builtin_amdgcn_raw_buffer_store_b64(v2u{F2U((lane & 2) ? ls : mx), thint}, rws, lane < 4 ? WS_X + wg * 128 + lane * 8 : OOB, 0, AUX_SC1);
    v2u om, os;
#pragma unroll 1
    for (int s = 0; s < SPIN_CAP; ++s) {
      __builtin_amdgcn_s_sleep(32);
      om = __builtin_amdgcn_raw_buffer_load_b64(rws, WS_X + (wg ^ 1) * 128 + (lane & 1) * 8, 0, AUX_SC1);
      os = __builtin_amdgcn_raw_buffer_load_b64(rws, WS_X + (wg ^ 1) * 128 + 16 + (lane & 1) * 8, 0, AUX_SC1);
      if (om.y == thint && os.y == thint) break;
      CBAR();
    }
    const float mt = fmaxf(mx, U2F(om.x));
    float la = ls * __builtin_amdgcn_exp2f((mx - mt) * LOG2E), lb = U2F(os.x) * __builtin_amdgcn_exp2f((U2F(om.x) - mt) * LOG2E);
    asm volatile("" : "+v"(la), "+v"(lb));
    if (lane < 4) FLG[F_LSEV + lane] = F2U((lane & 2) ? 1.f / (la + lb) : mt);
    CBAR();
    FLG[F_LSE] = 1u;
  }
  const v4u tg = TAG_FIELDS(tag);
  unsigned keep = ~0x3ffu;
  asm volatile("" : "+v"(keep));
#define WEIGHTS_LANDED() do { lds_wait_ge(FLG + F_FULL, 4u); lds_wait_ge(FLG + F_FULL + 1, 4u); } while (0)
  if (isO) {
    if (w >= 2) WEIGHTS_LANDED(); else WG_BARRIER();
    v4u af[16];
#pragma unroll
    for (int i = 0; i < 16; ++i) af[i] = trfrag(L + L_WO + ((w >> 1) * 256 + (i >> 3) * 128 + (w & 1) * 64 + ((i >> 2) & 1) * 32 + 8 * g + tq_) * 128 + (((i & 3) ^ f2) * 32) + tp * 8, 4 * 128);
    v4u pd[4];
#pragma unroll
    for (int i = 0; i < 4; ++i) pd[i] = *(LAS v4u*)(L + L_PAD + ((w & 1) * 4 + i) * 64 + g * 16);
    int rdst = L_WO + (w >> 1) * 32768 + (w & 1) * 8192 + n * 272 + g * 16, rsrc_ = L_WO + g * 32768 + w * 544 + n * 16;
    unsigned hdst = lane < 16 ? WS_H + ((w * 4 + sk) * 576 + sn * 64 + n * 4) * 4 : OOB;
    asm volatile("" : "+v"(rdst), "+v"(rsrc_), "+v"(hdst));
    if (w >= 2) WG_BARRIER();
    const int kv_ = sk * 4 + (w >> 1);
    v4u of[4];
#pragma unroll 1
    for (int s = 0; s < SPIN_CAP; ++s) {
      const unsigned cw = __builtin_amdgcn_raw_buffer_load_b32(rws, WS_CHK + (kv_ * 16 + n) * 8 + (w & 1) * 4, 0, AUX_SC1);
#pragma unroll
      for (int i = 0; i < 4; ++i) of[i] = __builtin_amdgcn_raw_buffer_load_b128(rws, WS_O + kv_ * 8192 + (w & 1) * 4096 + n * 64 + g * 16 + i * 1024, 0, AUX_SC1);
      const v4u xx = (of[0] ^ of[1]) ^ (of[2] ^ of[3]);
      const unsigned ck = xrow_xor((xx.x ^ xx.y) ^ (xx.z ^ xx.w)) ^ cw ^ thint;
      u64 bm = __ballot(ck != 0u);
      bm |= bm >> 32; bm |= bm >> 16;
      if (!((bm >> n) & 1u)) break;
      __builtin_amdgcn_s_sleep(1);
      CBAR();
    }
    if (sn == 0) {
      unsigned tk = 0;
      if (lane == 0) tk = __hip_atomic_fetch_add((LAS unsigned*)(FLG + F_GOT), 1u, __ATOMIC_RELAXED, __HIP_MEMORY_SCOPE_WORKGROUP);
      if (__builtin_amdgcn_readfirstlane(tk) == 7u) {
        __builtin_amdgcn_raw_buffer_store_b32(thint, rws, WS_HINT + lane * 128 + sk * 4, 0, AUX_SC1);
        __builtin_amdgcn_raw_buffer_store_b32(thint, rws, lane < 8 ? WS_HINT + 8192 + lane * 128 + sk * 4 : OOB, 0, AUX_SC1);
      }
    }
    f32x4 d[4];
#pragma unroll
    for (int t = 0; t < 4; ++t) d[t] = f32x4{0.f, 0.f, 0.f, 0.f};
#pragma unroll
    for (int ks = 0; ks < 2; ++ks) {
      const v4u c0 = of[2 * ks] ^ pd[2 * ks], c1 = of[2 * ks + 1] ^ pd[2 * ks + 1];
      const v4u b0 = {c0.x, c0.z, c1.x, c1.z}, b1 = {c0.y, c0.w, c1.y, c1.w};
#pragma unroll
      for (int t = 0; t < 4; ++t) d[t] = mfma32(af[ks * 4 + t], b0, d[t]);
#pragma unroll
      for (int t = 0; t < 4; ++t) d[t] = mfma32(af[8 + ks * 4 + t], b1, d[t]);
    }
#pragma unroll
    for (int t = 0; t < 4; ++t) *(LAS f32x4*)(L + rdst + t * 64) = d[t];
    WG_BARRIER();
    {
      f32x4 t = (*(LAS f32x4*)(L + rsrc_) + *(LAS f32x4*)(L + rsrc_ + 272)) + (*(LAS f32x4*)(L + rsrc_ + 8192) + *(LAS f32x4*)(L + rsrc_ + 8192 + 272));
      t.x = xrow_sum(t.x); t.y = xrow_sum(t.y); t.z = xrow_sum(t.z); t.w = xrow_sum(t.w);
      __builtin_amdgcn_raw_buffer_store_b128(tagged(t, tg, keep), rws, hdst, 0, AUX_SC1);
    }
  } else if (isF) {
    const bool dwave = w >= 2 && w < 7;
    const int dbk = w - 2;
    if (w >= 2) WEIGHTS_LANDED(); else WG_BARRIER();
    v2u xr[3];
    { const rsrc_t rx = mkrs(x, 8 * 576 * 2);
#pragma unroll
      for (int i = 0; i < 3; ++i) xr[i] = __builtin_amdgcn_raw_buffer_load_b64(rx, 8 * (live ? lane : 47) + 384 * i, w * 1152, 0); }
    v4u df[16];
#define DOWN_LOADS() do { const rsrc_t rd = mkrs(wdn, 4608 * 576 * 2); \
      const unsigned vo_ = (dbk < 4 || n < 8) ? (unsigned)(fu * 64 + 8 * g) * 1152u + dbk * 256 + n * 16 : OOB; \
      _Pragma("unroll") for (int i = 0; i < 16; ++i) df[i] = __builtin_amdgcn_raw_buffer_load_b128(rd, vo_, ((i >> 3) * 32 + (i & 7)) * 1152, AUX_NT); SB(); } while (0)
    v4u gf[18];
    { LAS unsigned char* ga = L + ((tp & 1) ? L_UP : L_GATE) + (8 * g + tq_) * 128 + ((w ^ (f2 | ((tp & 1) << 2))) * 16) + (tp >> 1) * 8;
#pragma unroll
      for (int ks = 0; ks < 18; ++ks) gf[ks] = trfrag(ga + ks * 32 * 128, 4 * 128); }
#pragma unroll
    for (int ks = 0; ks < 18; ++ks) asm volatile("" : "+v"(gf[ks]));
    int hoff[3];
#pragma unroll
    for (int i = 0; i < 3; ++i) { const int ll = live ? lane : 47, c = 24 * i + (ll >> 1); hoff[i] = L_XN + w * HN_STR + (c & 3) * 288 + (c >> 2) * 16 + (ll & 1) * 8; asm volatile("" : "+v"(hoff[i])); }
    int hsrc = WS_H + (w * 4 * 576 + 4 * (live ? lane : 47)) * 4, hbo = L_XN + (n & 7) * HN_STR + g * 288 + (n >> 3) * 144;
    int ado = L_ACT + min(n, 9) * ACT_STR + (8 * w + 2 * g) * 2 + (n == 8 ? ACT_STR : 0);
    int alo = L_ACT + (n < 8 ? n : 8) * ACT_STR + g * 16, ahi = L_ACT + (n < 8 ? 8 : n - 8) * ACT_STR + g * 16;
    asm volatile("" : "+v"(hsrc), "+v"(hbo), "+v"(ado), "+v"(alo), "+v"(ahi));
    if (w == 7 && lane < 36) *(LAS unsigned*)(L + L_ACT + 8 * ACT_STR + lane * 4) = 0u;
    unsigned po[4];
#pragma unroll
    for (int e = 0; e < 4; ++e) { po[e] = (dbk < 4 || g < 2) ? WS_P + ((32 * dbk + 8 * g + 2 * e + (n >> 3)) * 72 + fu) * 128 + (n & 7) * 16 : OOB; asm volatile("" : "+v"(po[e])); }
    if (w >= 2) { asm volatile("" : "+v"(xr[0]), "+v"(xr[1]), "+v"(xr[2])); WG_BARRIER(); if (dwave) DOWN_LOADS(); }
    if (dwave) {
#pragma unroll
      for (int ks = 0; ks < 2; ++ks) { v4u t_[8];
#pragma unroll
        for (int jj = 0; jj < 8; ++jj)
#pragma unroll
          for (int mm = 0; mm < 4; ++mm) t_[jj][mm] = __builtin_amdgcn_perm(df[ks * 8 + 2 * mm + 1][jj >> 1], df[ks * 8 + 2 * mm][jj >> 1], (jj & 1) ? 0x07060302u : 0x05040100u);
#pragma unroll
        for (int jj = 0; jj < 8; ++jj) { df[ks * 8 + jj] = t_[jj]; asm volatile("" : "+v"(df[ks * 8 + jj])); } }
    }
    f32x2 xf[6];
#pragma unroll
    for (int i = 0; i < 3; ++i) { xf[2 * i] = f32x2{bflo(xr[i].x), bfhi(xr[i].x)}; xf[2 * i + 1] = f32x2{bflo(xr[i].y), bfhi(xr[i].y)}; asm volatile("" : "+v"(xf[2 * i]), "+v"(xf[2 * i + 1])); }
    if (w == 7) {
#pragma unroll 1
      for (int s = 0; s < SPIN_CAP; ++s) {
        if (__builtin_amdgcn_raw_buffer_load_b32(rws, WS_HINT + fu * 128 + (lane & 3) * 4, 0, AUX_SC1) == thint) break;
        __builtin_amdgcn_s_sleep(2);
        CBAR();
      }
      __builtin_amdgcn_s_sleep(HP_DELAY);
    }
    WG_BARRIER();
    v4u hp[12];
#define H_LOAD1(i) hp[i] = __builtin_amdgcn_raw_buffer_load_b128(rws, hsrc, (i) * 768, AUX_SC1)
#pragma unroll
    for (int i = 0; i < 3; ++i) { H_LOAD1(i); H_LOAD1(3 + i); H_LOAD1(6 + i); H_LOAD1(9 + i); }
    SB();
    f32x2 hv[6]; float ref, s1, s2;
#pragma unroll 1
    for (int s = 0; s < SPIN_CAP; ++s) {
      unsigned bad[4] = {0u, 0u, 0u, 0u};
      f32x2 a1 = {0.f, 0.f}, a2 = {0.f, 0.f};
#pragma unroll
      for (int i = 0; i < 3; ++i) {
        v4u y[4];
#pragma unroll
        for (int q = 0; q < 4; ++q) { y[q] = hp[3 * q + i]; untag(y[q], tg, bad[q]); }
        const f32x4 a = (__builtin_bit_cast(f32x4, y[0]) + __builtin_bit_cast(f32x4, y[1])) + (__builtin_bit_cast(f32x4, y[2]) + __builtin_bit_cast(f32x4, y[3]));
        const unsigned r0 = cvtpk(a.x, a.y), r1 = cvtpk(a.z, a.w);
        const f32x2 u0 = xf[2 * i] + f32x2{bflo(r0), bfhi(r0)}, u1 = xf[2 * i + 1] + f32x2{bflo(r1), bfhi(r1)};
        const unsigned h0 = cvtpk(u0.x, u0.y), h1 = cvtpk(u1.x, u1.y);
        hv[2 * i] = f32x2{bflo(h0), bfhi(h0)}; hv[2 * i + 1] = f32x2{bflo(h1), bfhi(h1)};
        if (i == 0) ref = U2F(__builtin_amdgcn_readfirstlane(F2U(hv[0].x)));
        const f32x2 d0 = hv[2 * i] - ref, d1 = hv[2 * i + 1] - ref;
        a1 += d0; a2 = __builtin_elementwise_fma(d0, d0, a2); a1 += d1; a2 = __builtin_elementwise_fma(d1, d1, a2);
        asm volatile("" : "+v"(a1), "+v"(a2));
        SB();
      }
      s1 = a1.x + a1.y; s2 = a2.x + a2.y;
      if (!__any((((bad[0] | bad[1]) | (bad[2] | bad[3])) & 0x3ffu) != 0u)) break;
      CBAR();
#pragma unroll
      for (int q = 0; q < 4; ++q) if (__any((bad[q] & 0x3ffu) != 0u)) { H_LOAD1(3 * q); H_LOAD1(3 * q + 1); H_LOAD1(3 * q + 2); }
      __builtin_amdgcn_s_waitcnt(0x0f70);
    }
    { s1 = wave_sum(live ? s1 : 0.f); s2 = wave_sum(live ? s2 : 0.f);
      const float md = s1 * (1.f / 576.f);
      const float var = fmaxf(fmaf(-md, md, s2 * (1.f / 576.f)), 0.f);
      const float m = rbf(ref + md);
      const float inv = __builtin_amdgcn_rcpf(rbf(__builtin_amdgcn_sqrtf(rbf(rbf(var) + 1e-5f))));
#pragma unroll
      for (int i = 0; i < 3; ++i) {
        const f32x2 c0 = hv[2 * i] - m, c1 = hv[2 * i + 1] - m;
        const unsigned a = cvtpk(c0.x, c0.y), c = cvtpk(c1.x, c1.y);
        *(LAS v2u*)(L + hoff[i]) = v2u{cvtpk(bflo(a) * inv, bfhi(a) * inv), cvtpk(bflo(c) * inv, bfhi(c) * inv)};
      } }
    WG_BARRIER();
    {
      v4u hb[9];
#pragma unroll
      for (int ks = 0; ks < 9; ++ks) hb[ks] = *(LAS v4u*)(L + hbo + ks * 16);
      SB();
      f32x4 d = {0.f, 0.f, 0.f, 0.f}, d2 = {0.f, 0.f, 0.f, 0.f};
#pragma unroll
      for (int ks = 0; ks < 9; ++ks) { d = mfma32(gf[ks], hb[ks], d); d2 = mfma32(gf[9 + ks], hb[ks], d2); }
      d.x += dppf<ROR8>(d2.x); d.y += dppf<ROR8>(d2.y); d.z += dppf<ROR8>(d2.z); d.w += dppf<ROR8>(d2.w);
      auto s0 = __builtin_amdgcn_permlane16_swap(F2U(d.x), F2U(d.z), false, false);
      auto s1 = __builtin_amdgcn_permlane16_swap(F2U(d.y), F2U(d.w), false, false);
      float av[2];
#pragma unroll
      for (int e = 0; e < 2; ++e) {
        const float gg = rbf(U2F(e ? s1[0] : s0[0])), uu = rbf(U2F(e ? s1[1] : s0[1]));
        const float z2 = gg * fmaf(gg * gg, 0.044715f, 1.f) * (2.f * 0.7978845608028654f * LOG2E);
        av[e] = rbf(gg * __builtin_amdgcn_rcpf(1.f + __builtin_amdgcn_exp2f(-z2))) * uu; }
      *(LAS unsigned*)(L + ado) = cvtpk(av[0], av[1]); }
    WG_BARRIER();
    if (dwave) {
      const v4u l0 = *(LAS v4u*)(L + alo), h0 = *(LAS v4u*)(L + ahi), l1 = *(LAS v4u*)(L + alo + 64), h1 = *(LAS v4u*)(L + ahi + 64);
      f32x4 d[4];
#pragma unroll
      for (int jj = 0; jj < 4; ++jj) d[jj] = mfma32(df[jj], l0, f32x4{0.f, 0.f, 0.f, 0.f});
#pragma unroll
      for (int jj = 0; jj < 4; ++jj) d[jj] = mfma32(df[8 + jj], l1, d[jj]);
#pragma unroll
      for (int jj = 0; jj < 4; ++jj) d[jj] = mfma32(df[4 + jj], h0, d[jj]);
#pragma unroll
      for (int jj = 0; jj < 4; ++jj) d[jj] = mfma32(df[12 + jj], h1, d[jj]);
#pragma unroll
      for (int e = 0; e < 4; ++e)
        __builtin_amdgcn_raw_buffer_store_b128(tagged(f32x4{d[0][e], d[1][e], d[2][e], d[3][e]}, tg, keep), rws, po[e], 0, AUX_SC1);
    }
  } else {
    const int unit = rp < 108 ? rp : 108 + (rp - 108) * 2 + half;
    if (unit < 144) {
      const int ob_ = lane & 7, oc = unit * 4;
      WG_BARRIER();
      unsigned hh0 = 0, hh1 = 0;
      unsigned odst = lane < 8 ? (ob_ * 576 + oc) * 2 : OOB, edst = (unit == 0 && lane == 0) ? WS_EPOCH : OOB;
      asm volatile("" : "+v"(odst), "+v"(edst));
      if (w == 7) {
        const v2u xo = __builtin_amdgcn_raw_buffer_load_b64(mkrs(x, 8 * 576 * 2), (ob_ * 576 + oc) * 2, 0, 0);
        v4u hv;
#pragma unroll 1
        for (int s = 0; s < SPIN_CAP; ++s) {
          hv = __builtin_amdgcn_raw_buffer_load_b128(rws, WS_H + ((ob_ * 4 + ((lane >> 3) & 3)) * 576 + oc) * 4, 0, AUX_SC1);
          unsigned bad = 0; untag(hv, tg, bad);
          if ((bad & 0x3ffu) == 0u) break;
          __builtin_amdgcn_s_sleep(16);
          CBAR();
        }
        f32x4 a = __builtin_bit_cast(f32x4, hv);
        a.x += dppf<ROR8>(a.x); a.y += dppf<ROR8>(a.y); a.z += dppf<ROR8>(a.z); a.w += dppf<ROR8>(a.w);
        a.x = row2_sum(a.x); a.y = row2_sum(a.y); a.z = row2_sum(a.z); a.w = row2_sum(a.w);
        const unsigned r0 = cvtpk(a.x, a.y), r1 = cvtpk(a.z, a.w);
        hh0 = cvtpk(bflo(xo.x) + bflo(r0), bfhi(xo.x) + bfhi(r0)); hh1 = cvtpk(bflo(xo.y) + bflo(r1), bfhi(xo.y) + bfhi(r1));
      }
      WG_BARRIER();
      v4u p0, p1;
#pragma unroll 1
      for (int s = 0; s < SPIN_CAP; ++s) {
        p0 = __builtin_amdgcn_raw_buffer_load_b128(rws, WS_P + unit * 9216 + w * 1024 + lane * 16, 0, AUX_SC1);
        p1 = __builtin_amdgcn_raw_buffer_load_b128(rws, WS_P + unit * 9216 + 8192 + w * 128 + (lane & 7) * 16, 0, AUX_SC1);
        unsigned bad = 0; untag(p0, tg, bad); untag(p1, tg, bad);
        if ((bad & 0x3ffu) == 0u) break;
        __builtin_amdgcn_s_sleep(1);
        CBAR();
      }
      f32x4 t = __builtin_bit_cast(f32x4, p0);
      t.x += dppf<ROR8>(t.x); t.y += dppf<ROR8>(t.y); t.z += dppf<ROR8>(t.z); t.w += dppf<ROR8>(t.w);
      t.x = xrow_sum(t.x); t.y = xrow_sum(t.y); t.z = xrow_sum(t.z); t.w = xrow_sum(t.w);
      if (lane < 8) *(LAS f32x4*)(L + L_RED + w * 128 + lane * 16) = t + __builtin_bit_cast(f32x4, p1);
      WG_BARRIER();
      if (w == 7) {
        f32x4 f = *(LAS f32x4*)(L + L_RED + lane * 16);
        f.x += dppf<ROR8>(f.x); f.y += dppf<ROR8>(f.y); f.z += dppf<ROR8>(f.z); f.w += dppf<ROR8>(f.w);
        f.x = xrow_sum(f.x); f.y = xrow_sum(f.y); f.z = xrow_sum(f.z); f.w = xrow_sum(f.w);
        const unsigned f0 = cvtpk(f.x, f.y), f1 = cvtpk(f.z, f.w);
        __builtin_amdgcn_raw_buffer_store_b64(v2u{cvtpk(bflo(hh0) + bflo(f0), bfhi(hh0) + bfhi(f0)), cvtpk(bflo(hh1) + bflo(f1), bfhi(hh1) + bfhi(f1))},
            mkrs(out, 8 * 576 * 2), odst, 0, 0);
        __builtin_amdgcn_raw_buffer_store_b64(v2u{(unsigned)tag, thint}, rws, edst, 0, AUX_SC1);
      }
    }
  }
  asm volatile(".Lfb_code_end:");
}

extern "C" void kernel_launch(void* const* d_in, const int* in_sizes, int n_in, void* d_out, int out_size, void* d_ws, size_t ws_size, hipStream_t stream) {
  fused_block<<<256, 512, 0, stream>>>((const u16*)d_in[0], (const u16*)d_in[1], (const u16*)d_in[2], (const u16*)d_in[3], (const u16*)d_in[4],
      (const u16*)d_in[6], (const u16*)d_in[7], (const u16*)d_in[8], (u16*)d_out, (unsigned char*)d_ws);
}
```
